```python
import torch

def _softmax(x, axis=-1):
    return torch.softmax(x, dim=axis)
def _gelu(x):
    return torch.nn.functional.gelu(x, approximate="tanh")
def _silu(x):
    return torch.nn.functional.silu(x)
def _sigmoid(x):
    return torch.sigmoid(x)
def _layernorm(x, axis=-1, eps=1e-5):
    m = x.mean(dim=axis, keepdim=True)
    v = x.var(dim=axis, keepdim=True, unbiased=False)
    return (x - m) / torch.sqrt(v + eps)
def _scatter_add(tgt, idx, src, axis=0):
    return tgt.clone().index_add_(axis, idx, src)
def _segment_sum(src, seg, n):
    out = torch.zeros((n,) + src.shape[1:], dtype=src.dtype)
    return out.index_add_(0, seg, src)
def _pad(x, pw):
    flat = tuple(v for pair in reversed(pw) for v in pair)
    return torch.nn.functional.pad(x, flat)

kernel_name = 'standalone__gqa_softmax__near_ridge'

def setup_inputs():

    _w = (lambda a: (lambda t: t.to(torch.bfloat16) if t.is_floating_point() else t)(torch.as_tensor(a)))
    B, Hq, Hkv, Sq, Sk, D = 32, 32, 32, 192, 512, 128
    return {
        "q": _w((lambda *s: torch.randn(*s, dtype=torch.bfloat16) * 0.8)(B, Hq, Sq, D)),
        "k": _w((lambda *s: torch.randn(*s, dtype=torch.bfloat16) * 0.8)(B, Hkv, Sk, D)),
        "v": _w((lambda *s: torch.randn(*s, dtype=torch.bfloat16) * 0.05)(B, Hkv, Sk, D)),
    }

def reference(q, k, v):
    B, Hq, Sq, D = q.shape
    Hkv = k.shape[1]
    rep = Hq // Hkv
    k = (lambda x, n, axis: torch.repeat_interleave(x, n, dim=axis))(k, rep, axis=1)
    v = (lambda x, n, axis: torch.repeat_interleave(x, n, dim=axis))(v, rep, axis=1)
    scale = D ** -0.5
    scores = torch.einsum("bhqd,bhkd->bhqk", q, k) * scale
    p = _softmax(scores, axis=-1)
    return torch.einsum("bhqk,bhkd->bhqd", p, v)
```

```cpp
#include <hip/hip_runtime.h>
#include <hip/hip_bf16.h>

constexpr int B_ = 32, H_ = 32, SQ = 192, SK = 512, D = 128;
typedef __bf16 bf16x8 __attribute__((ext_vector_type(8)));
typedef __bf16 bf16x2 __attribute__((ext_vector_type(2)));
typedef float f32x4 __attribute__((ext_vector_type(4)));
typedef float f32x2 __attribute__((ext_vector_type(2)));
typedef unsigned u32x4 __attribute__((ext_vector_type(4)));
typedef unsigned u32x2 __attribute__((ext_vector_type(2)));
typedef unsigned short u16;

__device__ __forceinline__ unsigned cvtpk(float lo, float hi) {
  f32x2 v = {lo, hi}; bf16x2 b = __builtin_convertvector(v, bf16x2); return __builtin_bit_cast(unsigned, b);
}

__device__ __forceinline__ void slow_rows16(const u16* __restrict__ Q, const u16* __restrict__ K, const u16* __restrict__ V, u16* __restrict__ O, int lane) {
  const int c = lane & 15, g = lane >> 4;
  const float C = 0.12751743082f;
  bf16x8 qf[4];
#pragma unroll
  for (int s = 0; s < 4; ++s) qf[s] = *(const bf16x8*)(Q + c * D + 32 * s + 8 * g);
  float m = -__builtin_inff();
  for (int kb = 0; kb < SK / 16; ++kb) {
    f32x4 s4 = {0.f, 0.f, 0.f, 0.f};
#pragma unroll
    for (int s = 0; s < 4; ++s) {
      bf16x8 kf = *(const bf16x8*)(K + (16 * kb + c) * D + 32 * s + 8 * g);
      s4 = __builtin_amdgcn_mfma_f32_16x16x32_bf16(kf, qf[s], s4, 0, 0, 0);
    }
    m = fmaxf(m, fmaxf(fmaxf(s4[0], s4[1]), fmaxf(s4[2], s4[3])));
  }
  m = fmaxf(m, __shfl_xor(m, 16));
  m = fmaxf(m, __shfl_xor(m, 32));
  float l = 0.f;
  f32x4 o[8];
#pragma unroll
  for (int i = 0; i < 8; ++i) o[i] = f32x4{0.f, 0.f, 0.f, 0.f};
  for (int kb = 0; kb < SK / 32; ++kb) {
    f32x4 s0 = {0.f, 0.f, 0.f, 0.f}, s1 = {0.f, 0.f, 0.f, 0.f};
#pragma unroll
    for (int s = 0; s < 4; ++s) {
      bf16x8 k0 = *(const bf16x8*)(K + (32 * kb + c) * D + 32 * s + 8 * g);
      bf16x8 k1 = *(const bf16x8*)(K + (32 * kb + 16 + c) * D + 32 * s + 8 * g);
      s0 = __builtin_amdgcn_mfma_f32_16x16x32_bf16(k0, qf[s], s0, 0, 0, 0);
      s1 = __builtin_amdgcn_mfma_f32_16x16x32_bf16(k1, qf[s], s1, 0, 0, 0);
    }
#pragma unroll
    for (int r = 0; r < 4; ++r) { s0[r] = __builtin_amdgcn_exp2f((s0[r] - m) * C); s1[r] = __builtin_amdgcn_exp2f((s1[r] - m) * C); l += s0[r] + s1[r]; }
    u32x4 pw = {cvtpk(s0[0], s0[1]), cvtpk(s0[2], s0[3]), cvtpk(s1[0], s1[1]), cvtpk(s1[2], s1[3])};
    bf16x8 pf = __builtin_bit_cast(bf16x8, pw);
#pragma unroll
    for (int db = 0; db < 8; ++db) {
      const u16* vp = V + (32 * kb + 4 * g) * D + 16 * db + c;
      u32x4 vw = {(unsigned)vp[0] | ((unsigned)vp[D] << 16), (unsigned)vp[2 * D] | ((unsigned)vp[3 * D] << 16),
                  (unsigned)vp[16 * D] | ((unsigned)vp[17 * D] << 16), (unsigned)vp[18 * D] | ((unsigned)vp[19 * D] << 16)};
      o[db] = __builtin_amdgcn_mfma_f32_16x16x32_bf16(__builtin_bit_cast(bf16x8, vw), pf, o[db], 0, 0, 0);
    }
  }
  l += __shfl_xor(l, 16);
  l += __shfl_xor(l, 32);
  const float il = 1.0f / l;
#pragma unroll
  for (int db = 0; db < 8; ++db) {
    u32x2 w = {cvtpk(o[db][0] * il, o[db][1] * il), cvtpk(o[db][2] * il, o[db][3] * il)};
    *(u32x2*)(O + c * D + 16 * db + 4 * g) = w;
  }
}

__global__ __launch_bounds__(256) void attn_simple(const u16* __restrict__ Q, const u16* __restrict__ K, const u16* __restrict__ V, u16* __restrict__ O) {
  const int bh = blockIdx.x, wid = threadIdx.x >> 6, lane = threadIdx.x & 63;
  for (int qb = 0; qb < 3; ++qb) {
    const int row0 = wid * 48 + qb * 16;
    slow_rows16(Q + ((size_t)bh * SQ + row0) * D, K + (size_t)bh * SK * D, V + (size_t)bh * SK * D, O + ((size_t)bh * SQ + row0) * D, lane);
  }
}

extern "C" void kernel_launch(void* const* d_in, const int* in_sizes, int n_in, void* d_out, int out_size, void* d_ws, size_t ws_size, hipStream_t stream) {
  attn_simple<<<B_ * H_, 256, 0, stream>>>((const u16*)d_in[0], (const u16*)d_in[1], (const u16*)d_in[2], (u16*)d_out);
}
```

```cpp
#include <hip/hip_runtime.h>
#include <hip/hip_bf16.h>

constexpr int NHEADS = 1024, SQ = 192, SK = 512, D = 128, NWG = 256, HPW = NHEADS / NWG;
typedef __bf16 bf16x8 __attribute__((ext_vector_type(8)));
typedef __bf16 bf16x2 __attribute__((ext_vector_type(2)));
typedef float f32x4 __attribute__((ext_vector_type(4)));
typedef float f32x2 __attribute__((ext_vector_type(2)));
typedef unsigned u32x4 __attribute__((ext_vector_type(4)));
typedef unsigned u32x2 __attribute__((ext_vector_type(2)));
typedef unsigned short u16;

__device__ __forceinline__ unsigned cvtpk(float lo, float hi) {
  f32x2 v = {lo, hi}; bf16x2 b = __builtin_convertvector(v, bf16x2); return __builtin_bit_cast(unsigned, b);
}
__device__ __forceinline__ float lane_xor(float x, int lane, int mask) {
  return __builtin_bit_cast(float, __builtin_amdgcn_ds_bpermute((lane ^ mask) << 2, __builtin_bit_cast(int, x)));
}

__device__ __forceinline__ void exact_rows16(const u16* __restrict__ Q, const u16* __restrict__ K, const u16* __restrict__ V, u16* __restrict__ O, int lane) {
  const int c = lane & 15, g = lane >> 4;
  const float C = 0.12751743082f;
  bf16x8 qf[4];
#pragma unroll
  for (int s = 0; s < 4; ++s) qf[s] = *(const bf16x8*)(Q + c * D + 32 * s + 8 * g);
  float m = -__builtin_inff();
  for (int kb = 0; kb < SK / 16; ++kb) {
    f32x4 s4 = {0.f, 0.f, 0.f, 0.f};
#pragma unroll
    for (int s = 0; s < 4; ++s) {
      bf16x8 kf = *(const bf16x8*)(K + (16 * kb + c) * D + 32 * s + 8 * g);
      s4 = __builtin_amdgcn_mfma_f32_16x16x32_bf16(kf, qf[s], s4, 0, 0, 0);
    }
    m = fmaxf(m, fmaxf(fmaxf(s4[0], s4[1]), fmaxf(s4[2], s4[3])));
  }
  m = fmaxf(m, lane_xor(m, lane, 16));
  m = fmaxf(m, lane_xor(m, lane, 32));
  float l = 0.f;
  f32x4 o[8];
#pragma unroll
  for (int i = 0; i < 8; ++i) o[i] = f32x4{0.f, 0.f, 0.f, 0.f};
  for (int kb = 0; kb < SK / 32; ++kb) {
    f32x4 s0 = {0.f, 0.f, 0.f, 0.f}, s1 = {0.f, 0.f, 0.f, 0.f};
#pragma unroll
    for (int s = 0; s < 4; ++s) {
      bf16x8 k0 = *(const bf16x8*)(K + (32 * kb + c) * D + 32 * s + 8 * g);
      bf16x8 k1 = *(const bf16x8*)(K + (32 * kb + 16 + c) * D + 32 * s + 8 * g);
      s0 = __builtin_amdgcn_mfma_f32_16x16x32_bf16(k0, qf[s], s0, 0, 0, 0);
      s1 = __builtin_amdgcn_mfma_f32_16x16x32_bf16(k1, qf[s], s1, 0, 0, 0);
    }
#pragma unroll
    for (int r = 0; r < 4; ++r) { s0[r] = __builtin_amdgcn_exp2f((s0[r] - m) * C); s1[r] = __builtin_amdgcn_exp2f((s1[r] - m) * C); l += s0[r] + s1[r]; }
    u32x4 pw = {cvtpk(s0[0], s0[1]), cvtpk(s0[2], s0[3]), cvtpk(s1[0], s1[1]), cvtpk(s1[2], s1[3])};
    bf16x8 pf = __builtin_bit_cast(bf16x8, pw);
#pragma unroll
    for (int db = 0; db < 8; ++db) {
      const u16* vp = V + (32 * kb + 4 * g) * D + 16 * db + c;
      u32x4 vw = {(unsigned)vp[0] | ((unsigned)vp[D] << 16), (unsigned)vp[2 * D] | ((unsigned)vp[3 * D] << 16),
                  (unsigned)vp[16 * D] | ((unsigned)vp[17 * D] << 16), (unsigned)vp[18 * D] | ((unsigned)vp[19 * D] << 16)};
      o[db] = __builtin_amdgcn_mfma_f32_16x16x32_bf16(__builtin_bit_cast(bf16x8, vw), pf, o[db], 0, 0, 0);
    }
  }
  l += lane_xor(l, lane, 16);
  l += lane_xor(l, lane, 32);
  const float il = 1.0f / l;
#pragma unroll
  for (int db = 0; db < 8; ++db) {
    u32x2 w = {cvtpk(o[db][0] * il, o[db][1] * il), cvtpk(o[db][2] * il, o[db][3] * il)};
    *(u32x2*)(O + c * D + 16 * db + 4 * g) = w;
  }
}

__global__ __launch_bounds__(256, 1) void attn_fwd_stream(const u16* __restrict__ Q, const u16* __restrict__ K, const u16* __restrict__ V, u16* __restrict__ O
    ) {
  __shared__ __attribute__((aligned(16))) char lds[163840];
  const unsigned w = __builtin_amdgcn_readfirstlane(threadIdx.x >> 6), wg = blockIdx.x;
  const unsigned head0 = wg, rot = (((wg >> 3) & 7) * 2) & 15;
  unsigned flags;
  asm volatile(
    "s_load_dwordx8 s[68:75], %[ka], 0x0\n"
    "v_mbcnt_lo_u32_b32 v240, -1, 0\n"
    "v_mbcnt_hi_u32_b32 v240, -1, v240\n"
    "s_getpc_b64 s[94:95]\n"
    "Lpf_%=:\n"
    "s_lshl_b32 s67, %[w], 13\n"
    "v_lshl_add_u32 v245, v240, 7, s67\n"
    "v_min_u32 v245, Lend_%=-Lpf_%=-4, v245\n"
    "global_load_dword a196, v245, s[94:95]\n"
    "v_and_b32 v241, 15, v240\n"
    "v_lshrrev_b32 v242, 4, v240\n"
    "s_mul_i32 s58, %[w], 12288\n"
    "s_mul_i32 s67, %[wg], 49152\n"
    "v_xor_b32 v243, v241, v242\n"
    "v_lshlrev_b32 v244, 8, v242\n"
    "s_add_u32 s62, s58, 65536\n"
    "v_lshl_add_u32 v208, v243, 4, v244\n"
    "v_xor_b32 v209, 64, v208\n"
    "v_xor_b32 v210, 128, v208\n"
    "v_xor_b32 v211, 192, v208\n"
    "s_add_u32 s59, s58, 4096\n"
    "s_add_u32 s60, s58, 8192\n"
    "s_waitcnt lgkmcnt(0)\n"
    "s_add_u32 s48, s68, s67\n"
    "s_addc_u32 s49, s69, 0\n"
    "s_mov_b32 s50, 49152\n"
    "s_mov_b32 s51, 0x20000\n"
    "s_add_u32 m0, s62, 0\n"
    "s_nop 0\n"
    "buffer_load_dwordx4 v208, s[48:51], s58 offen offset:0 nt lds\n"
    "buffer_load_dwordx4 v209, s[48:51], s58 offen offset:1024 nt lds\n"
    "buffer_load_dwordx4 v210, s[48:51], s58 offen offset:2048 nt lds\n"
    "buffer_load_dwordx4 v211, s[48:51], s58 offen offset:3072 nt lds\n"
    "s_add_u32 m0, s62, 4096\n"
    "s_nop 0\n"
    "buffer_load_dwordx4 v208, s[48:51], s59 offen offset:0 nt lds\n"
    "buffer_load_dwordx4 v209, s[48:51], s59 offen offset:1024 nt lds\n"
    "buffer_load_dwordx4 v210, s[48:51], s59 offen offset:2048 nt lds\n"
    "buffer_load_dwordx4 v211, s[48:51], s59 offen offset:3072 nt lds\n"
    "s_add_u32 m0, s62, 8192\n"
    "s_nop 0\n"
    "buffer_load_dwordx4 v208, s[48:51], s60 offen offset:0 nt lds\n"
    "buffer_load_dwordx4 v209, s[48:51], s60 offen offset:1024 nt lds\n"
    "buffer_load_dwordx4 v210, s[48:51], s60 offen offset:2048 nt lds\n"
    "buffer_load_dwordx4 v211, s[48:51], s60 offen offset:3072 nt lds\n"
    "s_lshl_b32 s63, %[wg], 17\n"
    "s_add_u32 s40, s70, s63\n"
    "s_addc_u32 s41, s71, 0\n"
    "s_mov_b32 s42, 131072\n"
    "s_mov_b32 s43, 0x20000\n"
    "s_add_u32 s44, s72, s63\n"
    "s_addc_u32 s45, s73, 0\n"
    "s_mov_b32 s46, 131072\n"
    "s_mov_b32 s47, 0x20000\n"
    "s_lshl_b32 s56, %[rot], 13\n"
    "s_mov_b32 s57, s56\n"
    "s_lshl_b32 s61, %[w], 11\n"
    "s_and_b32 s63, %[w], 1\n"
    "s_lshl_b32 s63, s63, 7\n"
    "v_xor_b32 v204, s63, v208\n"
    "v_add_u32 v204, s61, v204\n"
    "v_xor_b32 v205, s63, v209\n"
    "v_add_u32 v205, s61, v205\n"
    "v_lshlrev_b32 v245, 1, v242\n"
    "v_xor_b32 v245, v245, v241\n"
    "v_lshl_add_u32 v245, v245, 4, v244\n"
    "v_add_u32 v206, s61, v245\n"
    "v_xor_b32 v207, 0x80, v206\n"
    "s_add_u32 m0, s61, 0\n"
    "s_nop 0\n"
    "buffer_load_dwordx4 v204, s[40:43], s56 offen offset:0 nt lds\n"
    "buffer_load_dwordx4 v205, s[40:43], s56 offen offset:1024 nt lds\n"
    "s_xor_b32 s56, s56, 8192\n"
    "s_add_u32 m0, s61, 8192\n"
    "s_nop 0\n"
    "buffer_load_dwordx4 v204, s[40:43], s56 offen offset:0 nt lds\n"
    "buffer_load_dwordx4 v205, s[40:43], s56 offen offset:1024 nt lds\n"
    "s_xor_b32 s56, s56, 24576\n"
    "s_add_u32 m0, s61, 32768\n"
    "s_nop 0\n"
    "buffer_load_dwordx4 v206, s[44:47], s57 offen offset:0 nt lds\n"
    "buffer_load_dwordx4 v207, s[44:47], s57 offen offset:1024 nt lds\n"
    "s_xor_b32 s57, s57, 8192\n"
    "s_add_u32 s52, s74, s67\n"
    "s_addc_u32 s53, s75, 0\n"
    "s_mov_b32 s54, 0\n"
    "s_mov_b32 s55, 0x20000\n"
    "s_sub_u32 s52, s52, 12582912\n"
    "s_subb_u32 s53, s53, 0\n"
    "s_mov_b32 s63, 0\n"
    "s_mov_b32 s64, 0\n"
    "s_mov_b32 s65, 0x3e0293ee\n"
    "s_mov_b32 s66, 0x4e800000\n"
    "s_mov_b32 s74, 0xffff0000\n"
    "v_lshlrev_b32 v246, 8, v241\n"
    "v_lshl_add_u32 v192, v243, 4, v246\n"
    "v_xor_b32 v193, 64, v192\n"
    "v_xor_b32 v194, 128, v192\n"
    "v_xor_b32 v195, 192, v192\n"
    "v_add_u32 v212, s62, v192\n"
    "v_add_u32 v213, s62, v193\n"
    "v_add_u32 v214, s62, v194\n"
    "v_add_u32 v215, s62, v195\n"
    "v_lshrrev_b32 v245, 2, v240\n"
    "v_and_b32 v247, 7, v245\n"
    "v_lshlrev_b32 v245, 8, v245\n"
    "v_lshl_add_u32 v245, v247, 5, v245\n"
    "v_and_b32 v247, 3, v240\n"
    "v_lshl_add_u32 v196, v247, 3, v245\n"
    "v_xor_b32 v197, 32, v196\n"
    "v_xor_b32 v198, 64, v196\n"
    "v_xor_b32 v199, 96, v196\n"
    "v_xor_b32 v200, 128, v196\n"
    "v_xor_b32 v201, 160, v196\n"
    "v_xor_b32 v202, 192, v196\n"
    "v_xor_b32 v203, 224, v196\n"
    "s_add_u32 s67, s58, 114688\n"
    "v_lshrrev_b32 v245, 1, v242\n"
    "v_and_b32 v247, 7, v241\n"
    "v_xor_b32 v245, v245, v247\n"
    "v_lshl_add_u32 v245, v245, 4, v246\n"
    "v_and_b32 v247, 1, v242\n"
    "v_lshl_add_u32 v245, v247, 3, v245\n"
    "v_xor_b32 v216, 0, v245\n"
    "v_add_u32 v216, s67, v216\n"
    "v_xor_b32 v217, 32, v245\n"
    "v_add_u32 v217, s67, v217\n"
    "v_xor_b32 v218, 64, v245\n"
    "v_add_u32 v218, s67, v218\n"
    "v_xor_b32 v219, 96, v245\n"
    "v_add_u32 v219, s67, v219\n"
    "v_add_u32 v220, s67, v208\n"
    "v_add_u32 v221, s67, v209\n"
    "v_lshlrev_b32 v222, 4, v240\n"
    "v_mov_b32 v245, 0x3f803f80\n"
    "v_and_b32 v247, 3, v240\n"
    "v_cmp_eq_u32 vcc, 0, v247\n"
    "s_nop 1\n"
    "v_cndmask_b32 v245, 0, v245, vcc\n"
    "v_accvgpr_write_b32 a112, v245\n"
    "v_accvgpr_write_b32 a113, v245\n"
    "v_accvgpr_write_b32 a114, v245\n"
    "v_accvgpr_write_b32 a115, v245\n"
    "s_mov_b32 s76, 0x4c525b70\n"
    "s_mov_b32 s77, 0x4748484d\n"
    "s_mov_b32 s78, 0x3e3e4848\n"
    "s_mov_b32 s79, 0x3e3e3f3e\n"
    "s_mov_b32 s80, 0x4e4e5762\n"
    "s_mov_b32 s81, 0x49494948\n"
    "s_mov_b32 s82, 0x3f3f4949\n"
    "s_mov_b32 s83, 0x60605f3f\n"
    "s_mov_b32 s84, 0x4f4f5962\n"
    "s_mov_b32 s85, 0x4b4a4b4a\n"
    "s_mov_b32 s86, 0x40414b4a\n"
    "s_mov_b32 s87, 0x62626241\n"
    "s_mov_b32 s88, 0x46454750\n"
    "s_mov_b32 s89, 0x40404141\n"
    "s_mov_b32 s90, 0x40404041\n"
    "s_mov_b32 s91, 0x37373837\n"
    "s_waitcnt vmcnt(14)\n"
    "ds_read_b128 v[224:227], v212 offset:0\n"
    "ds_read_b128 v[228:231], v213 offset:0\n"
    "ds_read_b128 v[232:235], v214 offset:0\n"
    "ds_read_b128 v[236:239], v215 offset:0\n"
    "s_waitcnt lgkmcnt(3)\n"
    "v_lshlrev_b32 v240, 16, v224\n"
    "v_lshlrev_b32 v242, 16, v225\n"
    "v_and_b32 v241, s74, v224\n"
    "v_and_b32 v243, s74, v225\n"
    "v_mul_f32 v240, s65, v240\n"
    "v_mul_f32 v242, s65, v242\n"
    "v_mul_f32 v241, s65, v241\n"
    "v_mul_f32 v243, s65, v243\n"
    "v_cvt_pk_bf16_f32 v240, v240, v241\n"
    "v_cvt_pk_bf16_f32 v242, v242, v243\n"
    "v_accvgpr_write_b32 a0, v240\n"
    "v_accvgpr_write_b32 a1, v242\n"
    "v_lshlrev_b32 v244, 16, v226\n"
    "v_lshlrev_b32 v246, 16, v227\n"
    "v_and_b32 v245, s74, v226\n"
    "v_and_b32 v247, s74, v227\n"
    "v_mul_f32 v244, s65, v244\n"
    "v_mul_f32 v246, s65, v246\n"
    "v_mul_f32 v245, s65, v245\n"
    "v_mul_f32 v247, s65, v247\n"
    "v_cvt_pk_bf16_f32 v244, v244, v245\n"
    "v_cvt_pk_bf16_f32 v246, v246, v247\n"
    "v_accvgpr_write_b32 a2, v244\n"
    "v_accvgpr_write_b32 a3, v246\n"
    "s_waitcnt lgkmcnt(0)\n"
    "v_lshlrev_b32 v240, 16, v228\n"
    "v_lshlrev_b32 v242, 16, v229\n"
    "v_and_b32 v241, s74, v228\n"
    "v_and_b32 v243, s74, v229\n"
    "v_mul_f32 v240, s65, v240\n"
    "v_mul_f32 v242, s65, v242\n"
    "v_mul_f32 v241, s65, v241\n"
    "v_mul_f32 v243, s65, v243\n"
    "v_cvt_pk_bf16_f32 v240, v240, v241\n"
    "v_cvt_pk_bf16_f32 v242, v242, v243\n"
    "v_accvgpr_write_b32 a4, v240\n"
    "v_accvgpr_write_b32 a5, v242\n"
    "v_lshlrev_b32 v244, 16, v230\n"
    "v_lshlrev_b32 v246, 16, v231\n"
    "v_and_b32 v245, s74, v230\n"
    "v_and_b32 v247, s74, v231\n"
    "v_mul_f32 v244, s65, v244\n"
    "v_mul_f32 v246, s65, v246\n"
    "v_mul_f32 v245, s65, v245\n"
    "v_mul_f32 v247, s65, v247\n"
    "v_cvt_pk_bf16_f32 v244, v244, v245\n"
    "v_cvt_pk_bf16_f32 v246, v246, v247\n"
    "v_accvgpr_write_b32 a6, v244\n"
    "v_accvgpr_write_b32 a7, v246\n"
    "v_lshlrev_b32 v240, 16, v232\n"
    "v_lshlrev_b32 v242, 16, v233\n"
    "v_and_b32 v241, s74, v232\n"
    "v_and_b32 v243, s74, v233\n"
    "v_mul_f32 v240, s65, v240\n"
    "v_mul_f32 v242, s65, v242\n"
    "v_mul_f32 v241, s65, v241\n"
    "v_mul_f32 v243, s65, v243\n"
    "v_cvt_pk_bf16_f32 v240, v240, v241\n"
    "v_cvt_pk_bf16_f32 v242, v242, v243\n"
    "v_accvgpr_write_b32 a8, v240\n"
    "v_accvgpr_write_b32 a9, v242\n"
    "v_lshlrev_b32 v244, 16, v234\n"
    "v_lshlrev_b32 v246, 16, v235\n"
    "v_and_b32 v245, s74, v234\n"
    "v_and_b32 v247, s74, v235\n"
    "v_mul_f32 v244, s65, v244\n"
    "v_mul_f32 v246, s65, v246\n"
    "v_mul_f32 v245, s65, v245\n"
    "v_mul_f32 v247, s65, v247\n"
    "v_cvt_pk_bf16_f32 v244, v244, v245\n"
    "v_cvt_pk_bf16_f32 v246, v246, v247\n"
    "v_accvgpr_write_b32 a10, v244\n"
    "v_accvgpr_write_b32 a11, v246\n"
    "v_lshlrev_b32 v240, 16, v236\n"
    "v_lshlrev_b32 v242, 16, v237\n"
    "v_and_b32 v241, s74, v236\n"
    "v_and_b32 v243, s74, v237\n"
    "v_mul_f32 v240, s65, v240\n"
    "v_mul_f32 v242, s65, v242\n"
    "v_mul_f32 v241, s65, v241\n"
    "v_mul_f32 v243, s65, v243\n"
    "v_cvt_pk_bf16_f32 v240, v240, v241\n"
    "v_cvt_pk_bf16_f32 v242, v242, v243\n"
    "v_accvgpr_write_b32 a12, v240\n"
    "v_accvgpr_write_b32 a13, v242\n"
    "v_lshlrev_b32 v244, 16, v238\n"
    "v_lshlrev_b32 v246, 16, v239\n"
    "v_and_b32 v245, s74, v238\n"
    "v_and_b32 v247, s74, v239\n"
    "v_mul_f32 v244, s65, v244\n"
    "v_mul_f32 v246, s65, v246\n"
    "v_mul_f32 v245, s65, v245\n"
    "v_mul_f32 v247, s65, v247\n"
    "v_cvt_pk_bf16_f32 v244, v244, v245\n"
    "v_cvt_pk_bf16_f32 v246, v246, v247\n"
    "v_accvgpr_write_b32 a14, v244\n"
    "v_accvgpr_write_b32 a15, v246\n"
    "s_add_u32 m0, s61, 16384\n"
    "s_nop 0\n"
    "buffer_load_dwordx4 v204, s[40:43], s56 offen offset:0 nt lds\n"
    "buffer_load_dwordx4 v205, s[40:43], s56 offen offset:1024 nt lds\n"
    "s_xor_b32 s56, s56, 8192\n"
    "s_waitcnt vmcnt(12)\n"
    "ds_read_b128 v[224:227], v212 offset:4096\n"
    "ds_read_b128 v[228:231], v213 offset:4096\n"
    "ds_read_b128 v[232:235], v214 offset:4096\n"
    "ds_read_b128 v[236:239], v215 offset:4096\n"
    "s_waitcnt lgkmcnt(3)\n"
    "v_lshlrev_b32 v240, 16, v224\n"
    "v_lshlrev_b32 v242, 16, v225\n"
    "v_and_b32 v241, s74, v224\n"
    "v_and_b32 v243, s74, v225\n"
    "v_mul_f32 v240, s65, v240\n"
    "v_mul_f32 v242, s65, v242\n"
    "v_mul_f32 v241, s65, v241\n"
    "v_mul_f32 v243, s65, v243\n"
    "v_cvt_pk_bf16_f32 v240, v240, v241\n"
    "v_cvt_pk_bf16_f32 v242, v242, v243\n"
    "v_accvgpr_write_b32 a16, v240\n"
    "v_accvgpr_write_b32 a17, v242\n"
    "v_lshlrev_b32 v244, 16, v226\n"
    "v_lshlrev_b32 v246, 16, v227\n"
    "v_and_b32 v245, s74, v226\n"
    "v_and_b32 v247, s74, v227\n"
    "v_mul_f32 v244, s65, v244\n"
    "v_mul_f32 v246, s65, v246\n"
    "v_mul_f32 v245, s65, v245\n"
    "v_mul_f32 v247, s65, v247\n"
    "v_cvt_pk_bf16_f32 v244, v244, v245\n"
    "v_cvt_pk_bf16_f32 v246, v246, v247\n"
    "v_accvgpr_write_b32 a18, v244\n"
    "v_accvgpr_write_b32 a19, v246\n"
    "s_waitcnt lgkmcnt(0)\n"
    "v_lshlrev_b32 v240, 16, v228\n"
    "v_lshlrev_b32 v242, 16, v229\n"
    "v_and_b32 v241, s74, v228\n"
    "v_and_b32 v243, s74, v229\n"
    "v_mul_f32 v240, s65, v240\n"
    "v_mul_f32 v242, s65, v242\n"
    "v_mul_f32 v241, s65, v241\n"
    "v_mul_f32 v243, s65, v243\n"
    "v_cvt_pk_bf16_f32 v240, v240, v241\n"
    "v_cvt_pk_bf16_f32 v242, v242, v243\n"
    "v_accvgpr_write_b32 a20, v240\n"
    "v_accvgpr_write_b32 a21, v242\n"
    "v_lshlrev_b32 v244, 16, v230\n"
    "v_lshlrev_b32 v246, 16, v231\n"
    "v_and_b32 v245, s74, v230\n"
    "v_and_b32 v247, s74, v231\n"
    "v_mul_f32 v244, s65, v244\n"
    "v_mul_f32 v246, s65, v246\n"
    "v_mul_f32 v245, s65, v245\n"
    "v_mul_f32 v247, s65, v247\n"
    "v_cvt_pk_bf16_f32 v244, v244, v245\n"
    "v_cvt_pk_bf16_f32 v246, v246, v247\n"
    "v_accvgpr_write_b32 a22, v244\n"
    "v_accvgpr_write_b32 a23, v246\n"
    "v_lshlrev_b32 v240, 16, v232\n"
    "v_lshlrev_b32 v242, 16, v233\n"
    "v_and_b32 v241, s74, v232\n"
    "v_and_b32 v243, s74, v233\n"
    "v_mul_f32 v240, s65, v240\n"
    "v_mul_f32 v242, s65, v242\n"
    "v_mul_f32 v241, s65, v241\n"
    "v_mul_f32 v243, s65, v243\n"
    "v_cvt_pk_bf16_f32 v240, v240, v241\n"
    "v_cvt_pk_bf16_f32 v242, v242, v243\n"
    "v_accvgpr_write_b32 a24, v240\n"
    "v_accvgpr_write_b32 a25, v242\n"
    "v_lshlrev_b32 v244, 16, v234\n"
    "v_lshlrev_b32 v246, 16, v235\n"
    "v_and_b32 v245, s74, v234\n"
    "v_and_b32 v247, s74, v235\n"
    "v_mul_f32 v244, s65, v244\n"
    "v_mul_f32 v246, s65, v246\n"
    "v_mul_f32 v245, s65, v245\n"
    "v_mul_f32 v247, s65, v247\n"
    "v_cvt_pk_bf16_f32 v244, v244, v245\n"
    "v_cvt_pk_bf16_f32 v246, v246, v247\n"
    "v_accvgpr_write_b32 a26, v244\n"
    "v_accvgpr_write_b32 a27, v246\n"
    "v_lshlrev_b32 v240, 16, v236\n"
    "v_lshlrev_b32 v242, 16, v237\n"
    "v_and_b32 v241, s74, v236\n"
    "v_and_b32 v243, s74, v237\n"
    "v_mul_f32 v240, s65, v240\n"
    "v_mul_f32 v242, s65, v242\n"
    "v_mul_f32 v241, s65, v241\n"
    "v_mul_f32 v243, s65, v243\n"
    "v_cvt_pk_bf16_f32 v240, v240, v241\n"
    "v_cvt_pk_bf16_f32 v242, v242, v243\n"
    "v_accvgpr_write_b32 a28, v240\n"
    "v_accvgpr_write_b32 a29, v242\n"
    "v_lshlrev_b32 v244, 16, v238\n"
    "v_lshlrev_b32 v246, 16, v239\n"
    "v_and_b32 v245, s74, v238\n"
    "v_and_b32 v247, s74, v239\n"
    "v_mul_f32 v244, s65, v244\n"
    "v_mul_f32 v246, s65, v246\n"
    "v_mul_f32 v245, s65, v245\n"
    "v_mul_f32 v247, s65, v247\n"
    "v_cvt_pk_bf16_f32 v244, v244, v245\n"
    "v_cvt_pk_bf16_f32 v246, v246, v247\n"
    "v_accvgpr_write_b32 a30, v244\n"
    "v_accvgpr_write_b32 a31, v246\n"
    "s_add_u32 m0, s61, 40960\n"
    "s_nop 0\n"
    "buffer_load_dwordx4 v206, s[44:47], s57 offen offset:0 nt lds\n"
    "buffer_load_dwordx4 v207, s[44:47], s57 offen offset:1024 nt lds\n"
    "s_xor_b32 s57, s57, 24576\n"
    "s_waitcnt vmcnt(10)\n"
    "ds_read_b128 v[224:227], v212 offset:8192\n"
    "ds_read_b128 v[228:231], v213 offset:8192\n"
    "ds_read_b128 v[232:235], v214 offset:8192\n"
    "ds_read_b128 v[236:239], v215 offset:8192\n"
    "s_waitcnt lgkmcnt(3)\n"
    "v_lshlrev_b32 v240, 16, v224\n"
    "v_lshlrev_b32 v242, 16, v225\n"
    "v_and_b32 v241, s74, v224\n"
    "v_and_b32 v243, s74, v225\n"
    "v_mul_f32 v240, s65, v240\n"
    "v_mul_f32 v242, s65, v242\n"
    "v_mul_f32 v241, s65, v241\n"
    "v_mul_f32 v243, s65, v243\n"
    "v_cvt_pk_bf16_f32 v240, v240, v241\n"
    "v_cvt_pk_bf16_f32 v242, v242, v243\n"
    "v_accvgpr_write_b32 a32, v240\n"
    "v_accvgpr_write_b32 a33, v242\n"
    "v_lshlrev_b32 v244, 16, v226\n"
    "v_lshlrev_b32 v246, 16, v227\n"
    "v_and_b32 v245, s74, v226\n"
    "v_and_b32 v247, s74, v227\n"
    "v_mul_f32 v244, s65, v244\n"
    "v_mul_f32 v246, s65, v246\n"
    "v_mul_f32 v245, s65, v245\n"
    "v_mul_f32 v247, s65, v247\n"
    "v_cvt_pk_bf16_f32 v244, v244, v245\n"
    "v_cvt_pk_bf16_f32 v246, v246, v247\n"
    "v_accvgpr_write_b32 a34, v244\n"
    "v_accvgpr_write_b32 a35, v246\n"
    "s_waitcnt lgkmcnt(0)\n"
    "v_lshlrev_b32 v240, 16, v228\n"
    "v_lshlrev_b32 v242, 16, v229\n"
    "v_and_b32 v241, s74, v228\n"
    "v_and_b32 v243, s74, v229\n"
    "v_mul_f32 v240, s65, v240\n"
    "v_mul_f32 v242, s65, v242\n"
    "v_mul_f32 v241, s65, v241\n"
    "v_mul_f32 v243, s65, v243\n"
    "v_cvt_pk_bf16_f32 v240, v240, v241\n"
    "v_cvt_pk_bf16_f32 v242, v242, v243\n"
    "v_accvgpr_write_b32 a36, v240\n"
    "v_accvgpr_write_b32 a37, v242\n"
    "v_lshlrev_b32 v244, 16, v230\n"
    "v_lshlrev_b32 v246, 16, v231\n"
    "v_and_b32 v245, s74, v230\n"
    "v_and_b32 v247, s74, v231\n"
    "v_mul_f32 v244, s65, v244\n"
    "v_mul_f32 v246, s65, v246\n"
    "v_mul_f32 v245, s65, v245\n"
    "v_mul_f32 v247, s65, v247\n"
    "v_cvt_pk_bf16_f32 v244, v244, v245\n"
    "v_cvt_pk_bf16_f32 v246, v246, v247\n"
    "v_accvgpr_write_b32 a38, v244\n"
    "v_accvgpr_write_b32 a39, v246\n"
    "v_lshlrev_b32 v240, 16, v232\n"
    "v_lshlrev_b32 v242, 16, v233\n"
    "v_and_b32 v241, s74, v232\n"
    "v_and_b32 v243, s74, v233\n"
    "v_mul_f32 v240, s65, v240\n"
    "v_mul_f32 v242, s65, v242\n"
    "v_mul_f32 v241, s65, v241\n"
    "v_mul_f32 v243, s65, v243\n"
    "v_cvt_pk_bf16_f32 v240, v240, v241\n"
    "v_cvt_pk_bf16_f32 v242, v242, v243\n"
    "v_accvgpr_write_b32 a40, v240\n"
    "v_accvgpr_write_b32 a41, v242\n"
    "v_lshlrev_b32 v244, 16, v234\n"
    "v_lshlrev_b32 v246, 16, v235\n"
    "v_and_b32 v245, s74, v234\n"
    "v_and_b32 v247, s74, v235\n"
    "v_mul_f32 v244, s65, v244\n"
    "v_mul_f32 v246, s65, v246\n"
    "v_mul_f32 v245, s65, v245\n"
    "v_mul_f32 v247, s65, v247\n"
    "v_cvt_pk_bf16_f32 v244, v244, v245\n"
    "v_cvt_pk_bf16_f32 v246, v246, v247\n"
    "v_accvgpr_write_b32 a42, v244\n"
    "v_accvgpr_write_b32 a43, v246\n"
    "v_lshlrev_b32 v240, 16, v236\n"
    "v_lshlrev_b32 v242, 16, v237\n"
    "v_and_b32 v241, s74, v236\n"
    "v_and_b32 v243, s74, v237\n"
    "v_mul_f32 v240, s65, v240\n"
    "v_mul_f32 v242, s65, v242\n"
    "v_mul_f32 v241, s65, v241\n"
    "v_mul_f32 v243, s65, v243\n"
    "v_cvt_pk_bf16_f32 v240, v240, v241\n"
    "v_cvt_pk_bf16_f32 v242, v242, v243\n"
    "v_accvgpr_write_b32 a44, v240\n"
    "v_accvgpr_write_b32 a45, v242\n"
    "v_lshlrev_b32 v244, 16, v238\n"
    "v_lshlrev_b32 v246, 16, v239\n"
    "v_and_b32 v245, s74, v238\n"
    "v_and_b32 v247, s74, v239\n"
    "v_mul_f32 v244, s65, v244\n"
    "v_mul_f32 v246, s65, v246\n"
    "v_mul_f32 v245, s65, v245\n"
    "v_mul_f32 v247, s65, v247\n"
    "v_cvt_pk_bf16_f32 v244, v244, v245\n"
    "v_cvt_pk_bf16_f32 v246, v246, v247\n"
    "v_accvgpr_write_b32 a46, v244\n"
    "v_accvgpr_write_b32 a47, v246\n"
    "s_add_u32 m0, s61, 24576\n"
    "s_nop 0\n"
    "buffer_load_dwordx4 v204, s[40:43], s56 offen offset:0 nt lds\n"
    "buffer_load_dwordx4 v205, s[40:43], s56 offen offset:1024 nt lds\n"
    "s_xor_b32 s56, s56, 57344\n"
    "s_waitcnt vmcnt(10) lgkmcnt(0)\n"
    "s_barrier\n"
    "ds_read_b128 a[48:51], v192 offset:0\n"
    "ds_read_b128 a[52:55], v193 offset:0\n"
    "ds_read_b128 a[56:59], v194 offset:0\n"
    "ds_read_b128 a[60:63], v195 offset:0\n"
    "ds_read_b128 a[64:67], v192 offset:4096\n"
    "ds_read_b128 a[68:71], v193 offset:4096\n"
    "ds_read_b128 a[72:75], v194 offset:4096\n"
    "ds_read_b128 a[76:79], v195 offset:4096\n"
    "s_waitcnt lgkmcnt(7)\n"
    "v_mfma_f32_16x16x32_bf16 v[120:123], a[48:51], a[0:3], 0\n"
    "s_add_u32 m0, s61, 49152\n"
    "s_waitcnt lgkmcnt(6)\n"
    "v_mfma_f32_16x16x32_bf16 v[120:123], a[52:55], a[4:7], v[120:123]\n"
    "s_waitcnt lgkmcnt(5)\n"
    "v_mfma_f32_16x16x32_bf16 v[120:123], a[56:59], a[8:11], v[120:123]\n"
    "s_waitcnt lgkmcnt(4)\n"
    "v_mfma_f32_16x16x32_bf16 v[120:123], a[60:63], a[12:15], v[120:123]\n"
    "s_waitcnt lgkmcnt(3)\n"
    "v_mfma_f32_16x16x32_bf16 v[124:127], a[64:67], a[0:3], 0\n"
    "s_waitcnt lgkmcnt(2)\n"
    "v_mfma_f32_16x16x32_bf16 v[124:127], a[68:71], a[4:7], v[124:127]\n"
    "s_waitcnt lgkmcnt(1)\n"
    "v_mfma_f32_16x16x32_bf16 v[124:127], a[72:75], a[8:11], v[124:127]\n"
    "buffer_load_dwordx4 v206, s[44:47], s57 offen offset:0 nt lds\n"
    "s_waitcnt lgkmcnt(0)\n"
    "v_mfma_f32_16x16x32_bf16 v[124:127], a[76:79], a[12:15], v[124:127]\n"
    "v_mfma_f32_16x16x32_bf16 v[128:131], a[48:51], a[16:19], 0\n"
    "v_mfma_f32_16x16x32_bf16 v[128:131], a[52:55], a[20:23], v[128:131]\n"
    "v_mfma_f32_16x16x32_bf16 v[128:131], a[56:59], a[24:27], v[128:131]\n"
    "v_mfma_f32_16x16x32_bf16 v[128:131], a[60:63], a[28:31], v[128:131]\n"
    "v_mfma_f32_16x16x32_bf16 v[132:135], a[64:67], a[16:19], 0\n"
    "v_mfma_f32_16x16x32_bf16 v[132:135], a[68:71], a[20:23], v[132:135]\n"
    "v_mfma_f32_16x16x32_bf16 v[132:135], a[72:75], a[24:27], v[132:135]\n"
    "v_mfma_f32_16x16x32_bf16 v[132:135], a[76:79], a[28:31], v[132:135]\n"
    "v_mfma_f32_16x16x32_bf16 v[136:139], a[48:51], a[32:35], 0\n"
    "v_mfma_f32_16x16x32_bf16 v[136:139], a[52:55], a[36:39], v[136:139]\n"
    "buffer_load_dwordx4 v207, s[44:47], s57 offen offset:1024 nt lds\n"
    "v_mfma_f32_16x16x32_bf16 v[136:139], a[56:59], a[40:43], v[136:139]\n"
    "s_xor_b32 s57, s57, 8192\n"
    "v_mfma_f32_16x16x32_bf16 v[136:139], a[60:63], a[44:47], v[136:139]\n"
    "s_memrealtime s[94:95]\n"
    "v_mfma_f32_16x16x32_bf16 v[140:143], a[64:67], a[32:35], 0\n"
    "v_mfma_f32_16x16x32_bf16 v[140:143], a[68:71], a[36:39], v[140:143]\n"
    "v_mfma_f32_16x16x32_bf16 v[140:143], a[72:75], a[40:43], v[140:143]\n"
    "v_mfma_f32_16x16x32_bf16 v[140:143], a[76:79], a[44:47], v[140:143]\n"
    "s_waitcnt vmcnt(8) lgkmcnt(0)\n"
    "s_bfe_u32 s93, s76, 0x80000\n"
    "s_add_u32 s97, s94, s93\n"
    "s_barrier\n"
    "ds_read_b128 a[48:51], v192 offset:8192\n"
    "ds_read_b128 a[52:55], v193 offset:8192\n"
    "ds_read_b128 a[56:59], v194 offset:8192\n"
    "ds_read_b128 a[60:63], v195 offset:8192\n"
    "ds_read_b128 a[64:67], v192 offset:12288\n"
    "ds_read_b128 a[68:71], v193 offset:12288\n"
    "ds_read_b128 a[72:75], v194 offset:12288\n"
    "ds_read_b128 a[76:79], v195 offset:12288\n"
    "v_max3_f32 v250, v120, v121, v122\n"
    "v_max3_f32 v252, v128, v129, v130\n"
    "v_max3_f32 v254, v136, v137, v138\n"
    "v_max3_f32 v251, v123, v124, v125\n"
    "v_max3_f32 v253, v131, v132, v133\n"
    "v_max3_f32 v255, v139, v140, v141\n"
    "v_max3_f32 v250, v250, v126, v127\n"
    "v_max3_f32 v252, v252, v134, v135\n"
    "v_max3_f32 v254, v254, v142, v143\n"
    "v_max_f32 v250, v250, v251\n"
    "v_max_f32 v252, v252, v253\n"
    "v_max_f32 v254, v254, v255\n"
    "v_mov_b32 v251, v250\n"
    "v_mov_b32 v253, v252\n"
    "v_mov_b32 v255, v254\n"
    "v_permlane16_swap_b32 v250, v251\n"
    "v_permlane16_swap_b32 v252, v253\n"
    "v_permlane16_swap_b32 v254, v255\n"
    "v_max_f32 v250, v250, v251\n"
    "v_max_f32 v252, v252, v253\n"
    "v_max_f32 v254, v254, v255\n"
    "v_mov_b32 v251, v250\n"
    "v_mov_b32 v253, v252\n"
    "v_mov_b32 v255, v254\n"
    "v_permlane32_swap_b32 v250, v251\n"
    "v_permlane32_swap_b32 v252, v253\n"
    "v_permlane32_swap_b32 v254, v255\n"
    "v_max_f32 v250, v250, v251\n"
    "v_max_f32 v252, v252, v253\n"
    "v_max_f32 v254, v254, v255\n"
    "v_sub_f32 v108, 0, v250\n"
    "v_sub_f32 v112, 0, v252\n"
    "v_sub_f32 v116, 0, v254\n"
    "v_sub_f32 v109, 0, v250\n"
    "v_sub_f32 v113, 0, v252\n"
    "v_sub_f32 v117, 0, v254\n"
    "v_sub_f32 v110, 0, v250\n"
    "v_sub_f32 v114, 0, v252\n"
    "v_sub_f32 v118, 0, v254\n"
    "v_sub_f32 v111, 0, v250\n"
    "v_sub_f32 v115, 0, v252\n"
    "v_sub_f32 v119, 0, v254\n"
    "s_waitcnt lgkmcnt(0)\n"
    "v_mfma_f32_16x16x32_bf16 v[144:147], a[48:51], a[0:3], v[108:111]\n"
    "s_add_u32 m0, s61, 57344\n"
    "ds_read_b64_tr_b16 a[80:81], v196 offset:32768\n"
    "v_sub_f32 v120, v120, v250\n"
    "v_exp_f32 v120, v120\n"
    "v_mfma_f32_16x16x32_bf16 v[144:147], a[52:55], a[4:7], v[144:147]\n"
    "ds_read_b64_tr_b16 a[82:83], v196 offset:36864\n"
    "v_sub_f32 v121, v121, v250\n"
    "v_exp_f32 v121, v121\n"
    "v_mfma_f32_16x16x32_bf16 v[144:147], a[56:59], a[8:11], v[144:147]\n"
    "buffer_load_dwordx4 v206, s[44:47], s57 offen offset:0 nt lds\n"
    "ds_read_b64_tr_b16 a[84:85], v197 offset:32768\n"
    "v_cvt_pk_bf16_f32 v168, v120, v121\n"
    "v_sub_f32 v122, v122, v250\n"
    "v_exp_f32 v122, v122\n"
    "v_mfma_f32_16x16x32_bf16 v[144:147], a[60:63], a[12:15], v[144:147]\n"
    "ds_read_b64_tr_b16 a[86:87], v197 offset:36864\n"
    "v_sub_f32 v123, v123, v250\n"
    "v_exp_f32 v123, v123\n"
    "v_mfma_f32_16x16x32_bf16 v[148:151], a[64:67], a[0:3], v[108:111]\n"
    "ds_read_b64_tr_b16 a[88:89], v198 offset:32768\n"
    "v_cvt_pk_bf16_f32 v169, v122, v123\n"
    "v_sub_f32 v124, v124, v250\n"
    "v_exp_f32 v124, v124\n"
    "v_mfma_f32_16x16x32_bf16 v[148:151], a[68:71], a[4:7], v[148:151]\n"
    "ds_read_b64_tr_b16 a[90:91], v198 offset:36864\n"
    "v_sub_f32 v125, v125, v250\n"
    "v_exp_f32 v125, v125\n"
    "v_mfma_f32_16x16x32_bf16 v[148:151], a[72:75], a[8:11], v[148:151]\n"
    "buffer_load_dwordx4 v207, s[44:47], s57 offen offset:1024 nt lds\n"
    "ds_read_b64_tr_b16 a[92:93], v199 offset:32768\n"
    "v_cvt_pk_bf16_f32 v170, v124, v125\n"
    "v_sub_f32 v126, v126, v250\n"
    "v_exp_f32 v126, v126\n"
    "v_mfma_f32_16x16x32_bf16 v[148:151], a[76:79], a[12:15], v[148:151]\n"
    "s_xor_b32 s57, s57, 57344\n"
    "ds_read_b64_tr_b16 a[94:95], v199 offset:36864\n"
    "v_sub_f32 v127, v127, v250\n"
    "v_exp_f32 v127, v127\n"
    "v_mfma_f32_16x16x32_bf16 v[152:155], a[48:51], a[16:19], v[112:115]\n"
    "s_add_u32 s48, s48, 12582912\n"
    "s_addc_u32 s49, s49, 0\n"
    "ds_read_b64_tr_b16 a[96:97], v200 offset:32768\n"
    "v_cvt_pk_bf16_f32 v171, v126, v127\n"
    "v_sub_f32 v128, v128, v252\n"
    "v_exp_f32 v128, v128\n"
    "v_mfma_f32_16x16x32_bf16 v[152:155], a[52:55], a[20:23], v[152:155]\n"
    "s_cmp_lt_u32 s63, 3\n"
    "s_cselect_b32 s50, 49152, 0\n"
    "s_add_u32 m0, s62, 0\n"
    "ds_read_b64_tr_b16 a[98:99], v200 offset:36864\n"
    "v_sub_f32 v129, v129, v252\n"
    "v_exp_f32 v129, v129\n"
    "v_mfma_f32_16x16x32_bf16 v[152:155], a[56:59], a[24:27], v[152:155]\n"
    "buffer_load_dwordx4 v208, s[48:51], s58 offen offset:0 nt lds\n"
    "ds_read_b64_tr_b16 a[100:101], v201 offset:32768\n"
    "v_cvt_pk_bf16_f32 v172, v128, v129\n"
    "v_sub_f32 v130, v130, v252\n"
    "v_exp_f32 v130, v130\n"
    "v_mfma_f32_16x16x32_bf16 v[152:155], a[60:63], a[28:31], v[152:155]\n"
    "s_add_u32 m0, s61, 0\n"
    "ds_read_b64_tr_b16 a[102:103], v201 offset:36864\n"
    "v_sub_f32 v131, v131, v252\n"
    "v_exp_f32 v131, v131\n"
    "v_mfma_f32_16x16x32_bf16 v[156:159], a[64:67], a[16:19], v[112:115]\n"
    "ds_read_b64_tr_b16 a[104:105], v202 offset:32768\n"
    "v_cvt_pk_bf16_f32 v173, v130, v131\n"
    "v_sub_f32 v132, v132, v252\n"
    "v_exp_f32 v132, v132\n"
    "v_mfma_f32_16x16x32_bf16 v[156:159], a[68:71], a[20:23], v[156:159]\n"
    "buffer_load_dwordx4 v204, s[40:43], s56 offen offset:0 nt lds\n"
    "ds_read_b64_tr_b16 a[106:107], v202 offset:36864\n"
    "v_sub_f32 v133, v133, v252\n"
    "v_exp_f32 v133, v133\n"
    "v_mfma_f32_16x16x32_bf16 v[156:159], a[72:75], a[24:27], v[156:159]\n"
    "ds_read_b64_tr_b16 a[108:109], v203 offset:32768\n"
    "v_cvt_pk_bf16_f32 v174, v132, v133\n"
    "v_sub_f32 v134, v134, v252\n"
    "v_exp_f32 v134, v134\n"
    "v_mfma_f32_16x16x32_bf16 v[156:159], a[76:79], a[28:31], v[156:159]\n"
    "ds_read_b64_tr_b16 a[110:111], v203 offset:36864\n"
    "v_sub_f32 v135, v135, v252\n"
    "v_exp_f32 v135, v135\n"
    "v_mfma_f32_16x16x32_bf16 v[160:163], a[48:51], a[32:35], v[116:119]\n"
    "v_cvt_pk_bf16_f32 v175, v134, v135\n"
    "v_sub_f32 v136, v136, v254\n"
    "v_exp_f32 v136, v136\n"
    "v_mfma_f32_16x16x32_bf16 v[160:163], a[52:55], a[36:39], v[160:163]\n"
    "buffer_load_dwordx4 v205, s[40:43], s56 offen offset:1024 nt lds\n"
    "v_sub_f32 v137, v137, v254\n"
    "v_exp_f32 v137, v137\n"
    "v_mfma_f32_16x16x32_bf16 v[160:163], a[56:59], a[40:43], v[160:163]\n"
    "s_xor_b32 s56, s56, 8192\n"
    "v_cvt_pk_bf16_f32 v176, v136, v137\n"
    "v_sub_f32 v138, v138, v254\n"
    "v_exp_f32 v138, v138\n"
    "v_mfma_f32_16x16x32_bf16 v[160:163], a[60:63], a[44:47], v[160:163]\n"
    "s_add_u32 m0, s62, 0\n"
    "v_sub_f32 v139, v139, v254\n"
    "v_exp_f32 v139, v139\n"
    "v_mfma_f32_16x16x32_bf16 v[164:167], a[64:67], a[32:35], v[116:119]\n"
    "s_memrealtime s[94:95]\n"
    "v_cvt_pk_bf16_f32 v177, v138, v139\n"
    "v_sub_f32 v140, v140, v254\n"
    "v_exp_f32 v140, v140\n"
    "v_mfma_f32_16x16x32_bf16 v[164:167], a[68:71], a[36:39], v[164:167]\n"
    "buffer_load_dwordx4 v209, s[48:51], s58 offen offset:1024 nt lds\n"
    "v_sub_f32 v141, v141, v254\n"
    "v_exp_f32 v141, v141\n"
    "v_mfma_f32_16x16x32_bf16 v[164:167], a[72:75], a[40:43], v[164:167]\n"
    "v_cvt_pk_bf16_f32 v178, v140, v141\n"
    "v_sub_f32 v142, v142, v254\n"
    "v_exp_f32 v142, v142\n"
    "v_mfma_f32_16x16x32_bf16 v[164:167], a[76:79], a[44:47], v[164:167]\n"
    "v_sub_f32 v143, v143, v254\n"
    "v_exp_f32 v143, v143\n"
    "s_nop 0\n"
    "v_cvt_pk_bf16_f32 v179, v142, v143\n"
    "s_waitcnt vmcnt(10) lgkmcnt(0)\n"
    "v_mfma_f32_16x16x32_bf16 v[96:99], a[112:115], v[168:171], 0\n"
    "Lpc0_%=:\n"
    "s_sub_u32 s96, s94, s97\n"
    "s_cmp_lt_i32 s96, 0\n"
    "s_cbranch_scc1 Lsp0_%=\n"
    "s_barrier\n"
    "ds_read_b128 a[48:51], v192 offset:16384\n"
    "ds_read_b128 a[52:55], v193 offset:16384\n"
    "v_mfma_f32_16x16x32_bf16 v[100:103], a[112:115], v[172:175], 0\n"
    "s_add_u32 m0, s61, 32768\n"
    "v_exp_f32 v144, v144\n"
    "v_mfma_f32_16x16x32_bf16 v[104:107], a[112:115], v[176:179], 0\n"
    "s_sub_u32 s96, s96, 60\n"
    "ds_read_b128 a[56:59], v194 offset:16384\n"
    "ds_read_b128 a[60:63], v195 offset:16384\n"
    "v_mfma_f32_16x16x32_bf16 v[0:3], a[80:83], v[168:171], 0\n"
    "s_max_i32 s96, s96, 0\n"
    "v_exp_f32 v145, v145\n"
    "v_mfma_f32_16x16x32_bf16 v[4:7], a[80:83], v[172:175], 0\n"
    "s_add_u32 s97, s97, s96\n"
    "buffer_load_dwordx4 v206, s[44:47], s57 offen offset:0 nt lds\n"
    "v_mfma_f32_16x16x32_bf16 v[8:11], a[80:83], v[176:179], 0\n"
    "s_bfe_u32 s93, s76, 0x80008\n"
    "v_exp_f32 v146, v146\n"
    "v_mfma_f32_16x16x32_bf16 v[12:15], a[84:87], v[168:171], 0\n"
    "s_add_u32 s97, s97, s93\n"
    "ds_read_b128 a[64:67], v192 offset:20480\n"
    "ds_read_b128 a[68:71], v193 offset:20480\n"
    "v_mfma_f32_16x16x32_bf16 v[16:19], a[84:87], v[172:175], 0\n"
    "v_exp_f32 v147, v147\n"
    "v_mfma_f32_16x16x32_bf16 v[20:23], a[84:87], v[176:179], 0\n"
    "ds_read_b128 a[72:75], v194 offset:20480\n"
    "ds_read_b128 a[76:79], v195 offset:20480\n"
    "v_mfma_f32_16x16x32_bf16 v[24:27], a[88:91], v[168:171], 0\n"
    "v_exp_f32 v148, v148\n"
    "v_mfma_f32_16x16x32_bf16 v[28:31], a[88:91], v[172:175], 0\n"
    "ds_read_b64_tr_b16 a[80:81], v196 offset:40960\n"
    "ds_read_b64_tr_b16 a[82:83], v196 offset:45056\n"
    "v_mfma_f32_16x16x32_bf16 v[32:35], a[88:91], v[176:179], 0\n"
    "v_exp_f32 v149, v149\n"
    "v_mfma_f32_16x16x32_bf16 v[36:39], a[92:95], v[168:171], 0\n"
    "buffer_load_dwordx4 v207, s[44:47], s57 offen offset:1024 nt lds\n"
    "v_mfma_f32_16x16x32_bf16 v[40:43], a[92:95], v[172:175], 0\n"
    "s_xor_b32 s57, s57, 8192\n"
    "v_exp_f32 v150, v150\n"
    "v_mfma_f32_16x16x32_bf16 v[44:47], a[92:95], v[176:179], 0\n"
    "s_add_u32 m0, s62, 0\n"
    "ds_read_b64_tr_b16 a[84:85], v197 offset:40960\n"
    "ds_read_b64_tr_b16 a[86:87], v197 offset:45056\n"
    "v_mfma_f32_16x16x32_bf16 v[48:51], a[96:99], v[168:171], 0\n"
    "v_exp_f32 v151, v151\n"
    "v_mfma_f32_16x16x32_bf16 v[52:55], a[96:99], v[172:175], 0\n"
    "ds_read_b64_tr_b16 a[88:89], v198 offset:40960\n"
    "v_cvt_pk_bf16_f32 v180, v144, v145\n"
    "v_mfma_f32_16x16x32_bf16 v[56:59], a[96:99], v[176:179], 0\n"
    "v_exp_f32 v152, v152\n"
    "v_mfma_f32_16x16x32_bf16 v[60:63], a[100:103], v[168:171], 0\n"
    "ds_read_b64_tr_b16 a[90:91], v198 offset:45056\n"
    "v_cvt_pk_bf16_f32 v181, v146, v147\n"
    "v_mfma_f32_16x16x32_bf16 v[64:67], a[100:103], v[172:175], 0\n"
    "v_exp_f32 v153, v153\n"
    "v_mfma_f32_16x16x32_bf16 v[68:71], a[100:103], v[176:179], 0\n"
    "buffer_load_dwordx4 v210, s[48:51], s58 offen offset:2048 nt lds\n"
    "v_mfma_f32_16x16x32_bf16 v[72:75], a[104:107], v[168:171], 0\n"
    "s_add_u32 m0, s61, 8192\n"
    "v_exp_f32 v154, v154\n"
    "v_mfma_f32_16x16x32_bf16 v[76:79], a[104:107], v[172:175], 0\n"
    "ds_read_b64_tr_b16 a[92:93], v199 offset:40960\n"
    "v_cvt_pk_bf16_f32 v182, v148, v149\n"
    "v_mfma_f32_16x16x32_bf16 v[80:83], a[104:107], v[176:179], 0\n"
    "v_exp_f32 v155, v155\n"
    "v_mfma_f32_16x16x32_bf16 v[84:87], a[108:111], v[168:171], 0\n"
    "ds_read_b64_tr_b16 a[94:95], v199 offset:45056\n"
    "v_cvt_pk_bf16_f32 v183, v150, v151\n"
    "v_mfma_f32_16x16x32_bf16 v[88:91], a[108:111], v[172:175], 0\n"
    "v_exp_f32 v156, v156\n"
    "v_mfma_f32_16x16x32_bf16 v[92:95], a[108:111], v[176:179], 0\n"
    "ds_read_b64_tr_b16 a[96:97], v200 offset:40960\n"
    "v_cvt_pk_bf16_f32 v184, v152, v153\n"
    "s_waitcnt lgkmcnt(4)\n"
    "v_mfma_f32_16x16x32_bf16 v[120:123], a[48:51], a[0:3], v[108:111]\n"
    "v_exp_f32 v157, v157\n"
    "v_mfma_f32_16x16x32_bf16 v[120:123], a[52:55], a[4:7], v[120:123]\n"
    "ds_read_b64_tr_b16 a[98:99], v200 offset:45056\n"
    "v_cvt_pk_bf16_f32 v185, v154, v155\n"
    "v_mfma_f32_16x16x32_bf16 v[120:123], a[56:59], a[8:11], v[120:123]\n"
    "v_exp_f32 v158, v158\n"
    "v_mfma_f32_16x16x32_bf16 v[120:123], a[60:63], a[12:15], v[120:123]\n"
    "buffer_load_dwordx4 v204, s[40:43], s56 offen offset:0 nt lds\n"
    "v_mfma_f32_16x16x32_bf16 v[124:127], a[64:67], a[0:3], v[108:111]\n"
    "v_exp_f32 v159, v159\n"
    "v_mfma_f32_16x16x32_bf16 v[124:127], a[68:71], a[4:7], v[124:127]\n"
    "ds_read_b64_tr_b16 a[100:101], v201 offset:40960\n"
    "v_cvt_pk_bf16_f32 v186, v156, v157\n"
    "v_mfma_f32_16x16x32_bf16 v[124:127], a[72:75], a[8:11], v[124:127]\n"
    "v_exp_f32 v160, v160\n"
    "v_mfma_f32_16x16x32_bf16 v[124:127], a[76:79], a[12:15], v[124:127]\n"
    "ds_read_b64_tr_b16 a[102:103], v201 offset:45056\n"
    "v_cvt_pk_bf16_f32 v187, v158, v159\n"
    "v_mfma_f32_16x16x32_bf16 v[128:131], a[48:51], a[16:19], v[112:115]\n"
    "v_exp_f32 v161, v161\n"
    "v_mfma_f32_16x16x32_bf16 v[128:131], a[52:55], a[20:23], v[128:131]\n"
    "ds_read_b64_tr_b16 a[104:105], v202 offset:40960\n"
    "v_cvt_pk_bf16_f32 v188, v160, v161\n"
    "v_mfma_f32_16x16x32_bf16 v[128:131], a[56:59], a[24:27], v[128:131]\n"
    "v_exp_f32 v162, v162\n"
    "v_mfma_f32_16x16x32_bf16 v[128:131], a[60:63], a[28:31], v[128:131]\n"
    "buffer_load_dwordx4 v205, s[40:43], s56 offen offset:1024 nt lds\n"
    "v_mfma_f32_16x16x32_bf16 v[132:135], a[64:67], a[16:19], v[112:115]\n"
    "s_xor_b32 s56, s56, 24576\n"
    "v_exp_f32 v163, v163\n"
    "v_mfma_f32_16x16x32_bf16 v[132:135], a[68:71], a[20:23], v[132:135]\n"
    "s_add_u32 m0, s62, 0\n"
    "ds_read_b64_tr_b16 a[106:107], v202 offset:45056\n"
    "v_cvt_pk_bf16_f32 v189, v162, v163\n"
    "v_mfma_f32_16x16x32_bf16 v[132:135], a[72:75], a[24:27], v[132:135]\n"
    "v_exp_f32 v164, v164\n"
    "v_mfma_f32_16x16x32_bf16 v[132:135], a[76:79], a[28:31], v[132:135]\n"
    "ds_read_b64_tr_b16 a[108:109], v203 offset:40960\n"
    "ds_read_b64_tr_b16 a[110:111], v203 offset:45056\n"
    "v_mfma_f32_16x16x32_bf16 v[136:139], a[48:51], a[32:35], v[116:119]\n"
    "v_exp_f32 v165, v165\n"
    "v_mfma_f32_16x16x32_bf16 v[136:139], a[52:55], a[36:39], v[136:139]\n"
    "v_cvt_pk_bf16_f32 v190, v164, v165\n"
    "v_mfma_f32_16x16x32_bf16 v[136:139], a[56:59], a[40:43], v[136:139]\n"
    "v_exp_f32 v166, v166\n"
    "v_mfma_f32_16x16x32_bf16 v[136:139], a[60:63], a[44:47], v[136:139]\n"
    "s_memrealtime s[94:95]\n"
    "buffer_load_dwordx4 v211, s[48:51], s58 offen offset:3072 nt lds\n"
    "v_mfma_f32_16x16x32_bf16 v[140:143], a[64:67], a[32:35], v[116:119]\n"
    "v_exp_f32 v167, v167\n"
    "v_mfma_f32_16x16x32_bf16 v[140:143], a[68:71], a[36:39], v[140:143]\n"
    "v_cvt_pk_bf16_f32 v191, v166, v167\n"
    "v_mfma_f32_16x16x32_bf16 v[140:143], a[72:75], a[40:43], v[140:143]\n"
    "v_mfma_f32_16x16x32_bf16 v[140:143], a[76:79], a[44:47], v[140:143]\n"
    "Lloop_%=:\n"
    "s_waitcnt vmcnt(12) lgkmcnt(0)\n"
    "v_mfma_f32_16x16x32_bf16 v[96:99], a[112:115], v[180:183], v[96:99]\n"
    "Lpc1_%=:\n"
    "s_sub_u32 s96, s94, s97\n"
    "s_cmp_lt_i32 s96, 0\n"
    "s_cbranch_scc1 Lsp1_%=\n"
    "s_barrier\n"
    "ds_read_b128 a[48:51], v192 offset:24576\n"
    "ds_read_b128 a[52:55], v193 offset:24576\n"
    "v_mfma_f32_16x16x32_bf16 v[100:103], a[112:115], v[184:187], v[100:103]\n"
    "s_add_u32 m0, s61, 40960\n"
    "v_exp_f32 v120, v120\n"
    "v_mfma_f32_16x16x32_bf16 v[104:107], a[112:115], v[188:191], v[104:107]\n"
    "s_sub_u32 s96, s96, 60\n"
    "ds_read_b128 a[56:59], v194 offset:24576\n"
    "ds_read_b128 a[60:63], v195 offset:24576\n"
    "v_mfma_f32_16x16x32_bf16 v[0:3], a[80:83], v[180:183], v[0:3]\n"
    "s_max_i32 s96, s96, 0\n"
    "v_exp_f32 v121, v121\n"
    "v_mfma_f32_16x16x32_bf16 v[4:7], a[80:83], v[184:187], v[4:7]\n"
    "s_add_u32 s97, s97, s96\n"
    "buffer_load_dwordx4 v206, s[44:47], s57 offen offset:0 nt lds\n"
    "v_mfma_f32_16x16x32_bf16 v[8:11], a[80:83], v[188:191], v[8:11]\n"
    "s_bfe_u32 s93, s76, 0x80010\n"
    "v_exp_f32 v122, v122\n"
    "v_mfma_f32_16x16x32_bf16 v[12:15], a[84:87], v[180:183], v[12:15]\n"
    "s_add_u32 s97, s97, s93\n"
    "ds_read_b128 a[64:67], v192 offset:28672\n"
    "ds_read_b128 a[68:71], v193 offset:28672\n"
    "v_mfma_f32_16x16x32_bf16 v[16:19], a[84:87], v[184:187], v[16:19]\n"
    "v_exp_f32 v123, v123\n"
    "v_mfma_f32_16x16x32_bf16 v[20:23], a[84:87], v[188:191], v[20:23]\n"
    "ds_read_b128 a[72:75], v194 offset:28672\n"
    "ds_read_b128 a[76:79], v195 offset:28672\n"
    "v_mfma_f32_16x16x32_bf16 v[24:27], a[88:91], v[180:183], v[24:27]\n"
    "v_exp_f32 v124, v124\n"
    "v_mfma_f32_16x16x32_bf16 v[28:31], a[88:91], v[184:187], v[28:31]\n"
    "ds_read_b64_tr_b16 a[80:81], v196 offset:49152\n"
    "ds_read_b64_tr_b16 a[82:83], v196 offset:53248\n"
    "v_mfma_f32_16x16x32_bf16 v[32:35], a[88:91], v[188:191], v[32:35]\n"
    "v_exp_f32 v125, v125\n"
    "v_mfma_f32_16x16x32_bf16 v[36:39], a[92:95], v[180:183], v[36:39]\n"
    "ds_read_b64_tr_b16 a[84:85], v197 offset:49152\n"
    "ds_read_b64_tr_b16 a[86:87], v197 offset:53248\n"
    "v_mfma_f32_16x16x32_bf16 v[40:43], a[92:95], v[184:187], v[40:43]\n"
    "v_exp_f32 v126, v126\n"
    "v_mfma_f32_16x16x32_bf16 v[44:47], a[92:95], v[188:191], v[44:47]\n"
    "buffer_load_dwordx4 v207, s[44:47], s57 offen offset:1024 nt lds\n"
    "v_mfma_f32_16x16x32_bf16 v[48:51], a[96:99], v[180:183], v[48:51]\n"
    "s_xor_b32 s57, s57, 24576\n"
    "v_exp_f32 v127, v127\n"
    "v_mfma_f32_16x16x32_bf16 v[52:55], a[96:99], v[184:187], v[52:55]\n"
    "s_add_u32 m0, s62, 4096\n"
    "ds_read_b64_tr_b16 a[88:89], v198 offset:49152\n"
    "v_cvt_pk_bf16_f32 v168, v120, v121\n"
    "v_mfma_f32_16x16x32_bf16 v[56:59], a[96:99], v[188:191], v[56:59]\n"
    "v_exp_f32 v128, v128\n"
    "v_mfma_f32_16x16x32_bf16 v[60:63], a[100:103], v[180:183], v[60:63]\n"
    "ds_read_b64_tr_b16 a[90:91], v198 offset:53248\n"
    "v_cvt_pk_bf16_f32 v169, v122, v123\n"
    "v_mfma_f32_16x16x32_bf16 v[64:67], a[100:103], v[184:187], v[64:67]\n"
    "v_exp_f32 v129, v129\n"
    "v_mfma_f32_16x16x32_bf16 v[68:71], a[100:103], v[188:191], v[68:71]\n"
    "ds_read_b64_tr_b16 a[92:93], v199 offset:49152\n"
    "v_cvt_pk_bf16_f32 v170, v124, v125\n"
    "v_mfma_f32_16x16x32_bf16 v[72:75], a[104:107], v[180:183], v[72:75]\n"
    "v_exp_f32 v130, v130\n"
    "v_mfma_f32_16x16x32_bf16 v[76:79], a[104:107], v[184:187], v[76:79]\n"
    "ds_read_b64_tr_b16 a[94:95], v199 offset:53248\n"
    "v_cvt_pk_bf16_f32 v171, v126, v127\n"
    "v_mfma_f32_16x16x32_bf16 v[80:83], a[104:107], v[188:191], v[80:83]\n"
    "v_exp_f32 v131, v131\n"
    "v_mfma_f32_16x16x32_bf16 v[84:87], a[108:111], v[180:183], v[84:87]\n"
    "buffer_load_dwordx4 v208, s[48:51], s59 offen offset:0 nt lds\n"
    "v_mfma_f32_16x16x32_bf16 v[88:91], a[108:111], v[184:187], v[88:91]\n"
    "s_add_u32 m0, s61, 16384\n"
    "v_exp_f32 v132, v132\n"
    "v_mfma_f32_16x16x32_bf16 v[92:95], a[108:111], v[188:191], v[92:95]\n"
    "ds_read_b64_tr_b16 a[96:97], v200 offset:49152\n"
    "v_cvt_pk_bf16_f32 v172, v128, v129\n"
    "s_waitcnt lgkmcnt(4)\n"
    "v_mfma_f32_16x16x32_bf16 v[144:147], a[48:51], a[0:3], v[108:111]\n"
    "v_exp_f32 v133, v133\n"
    "v_mfma_f32_16x16x32_bf16 v[144:147], a[52:55], a[4:7], v[144:147]\n"
    "ds_read_b64_tr_b16 a[98:99], v200 offset:53248\n"
    "v_cvt_pk_bf16_f32 v173, v130, v131\n"
    "v_mfma_f32_16x16x32_bf16 v[144:147], a[56:59], a[8:11], v[144:147]\n"
    "v_exp_f32 v134, v134\n"
    "v_mfma_f32_16x16x32_bf16 v[144:147], a[60:63], a[12:15], v[144:147]\n"
    "ds_read_b64_tr_b16 a[100:101], v201 offset:49152\n"
    "v_cvt_pk_bf16_f32 v174, v132, v133\n"
    "v_mfma_f32_16x16x32_bf16 v[148:151], a[64:67], a[0:3], v[108:111]\n"
    "v_exp_f32 v135, v135\n"
    "v_mfma_f32_16x16x32_bf16 v[148:151], a[68:71], a[4:7], v[148:151]\n"
    "ds_read_b64_tr_b16 a[102:103], v201 offset:53248\n"
    "v_cvt_pk_bf16_f32 v175, v134, v135\n"
    "v_mfma_f32_16x16x32_bf16 v[148:151], a[72:75], a[8:11], v[148:151]\n"
    "v_exp_f32 v136, v136\n"
    "v_mfma_f32_16x16x32_bf16 v[148:151], a[76:79], a[12:15], v[148:151]\n"
    "buffer_load_dwordx4 v204, s[40:43], s56 offen offset:0 nt lds\n"
    "v_mfma_f32_16x16x32_bf16 v[152:155], a[48:51], a[16:19], v[112:115]\n"
    "v_exp_f32 v137, v137\n"
    "v_mfma_f32_16x16x32_bf16 v[152:155], a[52:55], a[20:23], v[152:155]\n"
    "ds_read_b64_tr_b16 a[104:105], v202 offset:49152\n"
    "v_cvt_pk_bf16_f32 v176, v136, v137\n"
    "v_mfma_f32_16x16x32_bf16 v[152:155], a[56:59], a[24:27], v[152:155]\n"
    "v_exp_f32 v138, v138\n"
    "v_mfma_f32_16x16x32_bf16 v[152:155], a[60:63], a[28:31], v[152:155]\n"
    "ds_read_b64_tr_b16 a[106:107], v202 offset:53248\n"
    "ds_read_b64_tr_b16 a[108:109], v203 offset:49152\n"
    "v_mfma_f32_16x16x32_bf16 v[156:159], a[64:67], a[16:19], v[112:115]\n"
    "v_exp_f32 v139, v139\n"
    "v_mfma_f32_16x16x32_bf16 v[156:159], a[68:71], a[20:23], v[156:159]\n"
    "ds_read_b64_tr_b16 a[110:111], v203 offset:53248\n"
    "v_cvt_pk_bf16_f32 v177, v138, v139\n"
    "v_mfma_f32_16x16x32_bf16 v[156:159], a[72:75], a[24:27], v[156:159]\n"
    "v_exp_f32 v140, v140\n"
    "v_mfma_f32_16x16x32_bf16 v[156:159], a[76:79], a[28:31], v[156:159]\n"
    "ds_read_b128 a[116:119], v220 offset:0\n"
    "ds_read_b128 a[120:123], v221 offset:1024\n"
    "v_mfma_f32_16x16x32_bf16 v[160:163], a[48:51], a[32:35], v[116:119]\n"
    "v_exp_f32 v141, v141\n"
    "v_mfma_f32_16x16x32_bf16 v[160:163], a[52:55], a[36:39], v[160:163]\n"
    "buffer_load_dwordx4 v205, s[40:43], s56 offen offset:1024 nt lds\n"
    "v_mfma_f32_16x16x32_bf16 v[160:163], a[56:59], a[40:43], v[160:163]\n"
    "s_xor_b32 s56, s56, 8192\n"
    "v_exp_f32 v142, v142\n"
    "v_mfma_f32_16x16x32_bf16 v[160:163], a[60:63], a[44:47], v[160:163]\n"
    "s_memrealtime s[94:95]\n"
    "v_cvt_pk_bf16_f32 v178, v140, v141\n"
    "v_mfma_f32_16x16x32_bf16 v[164:167], a[64:67], a[32:35], v[116:119]\n"
    "v_exp_f32 v143, v143\n"
    "v_mfma_f32_16x16x32_bf16 v[164:167], a[68:71], a[36:39], v[164:167]\n"
    "v_cvt_pk_bf16_f32 v179, v142, v143\n"
    "v_mfma_f32_16x16x32_bf16 v[164:167], a[72:75], a[40:43], v[164:167]\n"
    "v_mfma_f32_16x16x32_bf16 v[164:167], a[76:79], a[44:47], v[164:167]\n"
    "s_waitcnt vmcnt(12) lgkmcnt(0)\n"
    "v_mfma_f32_16x16x32_bf16 v[96:99], a[112:115], v[168:171], v[96:99]\n"
    "Lpc2_%=:\n"
    "s_sub_u32 s96, s94, s97\n"
    "s_cmp_lt_i32 s96, 0\n"
    "s_cbranch_scc1 Lsp2_%=\n"
    "s_barrier\n"
    "ds_read_b128 a[48:51], v192 offset:0\n"
    "ds_read_b128 a[52:55], v193 offset:0\n"
    "v_mfma_f32_16x16x32_bf16 v[100:103], a[112:115], v[172:175], v[100:103]\n"
    "s_add_u32 m0, s61, 49152\n"
    "v_exp_f32 v144, v144\n"
    "v_mfma_f32_16x16x32_bf16 v[104:107], a[112:115], v[176:179], v[104:107]\n"
    "s_sub_u32 s96, s96, 60\n"
    "ds_read_b128 a[56:59], v194 offset:0\n"
    "ds_read_b128 a[60:63], v195 offset:0\n"
    "v_mfma_f32_16x16x32_bf16 v[0:3], a[80:83], v[168:171], v[0:3]\n"
    "s_max_i32 s96, s96, 0\n"
    "v_exp_f32 v145, v145\n"
    "v_mfma_f32_16x16x32_bf16 v[4:7], a[80:83], v[172:175], v[4:7]\n"
    "s_add_u32 s97, s97, s96\n"
    "buffer_load_dwordx4 v206, s[44:47], s57 offen offset:0 nt lds\n"
    "v_mfma_f32_16x16x32_bf16 v[8:11], a[80:83], v[176:179], v[8:11]\n"
    "s_bfe_u32 s93, s76, 0x80018\n"
    "v_exp_f32 v146, v146\n"
    "v_mfma_f32_16x16x32_bf16 v[12:15], a[84:87], v[168:171], v[12:15]\n"
    "s_add_u32 s97, s97, s93\n"
    "ds_read_b128 a[64:67], v192 offset:4096\n"
    "ds_read_b128 a[68:71], v193 offset:4096\n"
    "v_mfma_f32_16x16x32_bf16 v[16:19], a[84:87], v[172:175], v[16:19]\n"
    "v_exp_f32 v147, v147\n"
    "v_mfma_f32_16x16x32_bf16 v[20:23], a[84:87], v[176:179], v[20:23]\n"
    "ds_read_b128 a[72:75], v194 offset:4096\n"
    "ds_read_b128 a[76:79], v195 offset:4096\n"
    "v_mfma_f32_16x16x32_bf16 v[24:27], a[88:91], v[168:171], v[24:27]\n"
    "v_exp_f32 v148, v148\n"
    "v_mfma_f32_16x16x32_bf16 v[28:31], a[88:91], v[172:175], v[28:31]\n"
    "ds_read_b64_tr_b16 a[80:81], v196 offset:57344\n"
    "ds_read_b64_tr_b16 a[82:83], v196 offset:61440\n"
    "v_mfma_f32_16x16x32_bf16 v[32:35], a[88:91], v[176:179], v[32:35]\n"
    "v_exp_f32 v149, v149\n"
    "v_mfma_f32_16x16x32_bf16 v[36:39], a[92:95], v[168:171], v[36:39]\n"
    "ds_read_b64_tr_b16 a[84:85], v197 offset:57344\n"
    "ds_read_b64_tr_b16 a[86:87], v197 offset:61440\n"
    "v_mfma_f32_16x16x32_bf16 v[40:43], a[92:95], v[172:175], v[40:43]\n"
    "v_exp_f32 v150, v150\n"
    "v_mfma_f32_16x16x32_bf16 v[44:47], a[92:95], v[176:179], v[44:47]\n"
    "buffer_load_dwordx4 v207, s[44:47], s57 offen offset:1024 nt lds\n"
    "v_mfma_f32_16x16x32_bf16 v[48:51], a[96:99], v[168:171], v[48:51]\n"
    "s_xor_b32 s57, s57, 8192\n"
    "v_exp_f32 v151, v151\n"
    "v_mfma_f32_16x16x32_bf16 v[52:55], a[96:99], v[172:175], v[52:55]\n"
    "s_add_u32 m0, s62, 4096\n"
    "ds_read_b64_tr_b16 a[88:89], v198 offset:57344\n"
    "v_cvt_pk_bf16_f32 v180, v144, v145\n"
    "v_mfma_f32_16x16x32_bf16 v[56:59], a[96:99], v[176:179], v[56:59]\n"
    "v_exp_f32 v152, v152\n"
    "v_mfma_f32_16x16x32_bf16 v[60:63], a[100:103], v[168:171], v[60:63]\n"
    "ds_read_b64_tr_b16 a[90:91], v198 offset:61440\n"
    "v_cvt_pk_bf16_f32 v181, v146, v147\n"
    "v_mfma_f32_16x16x32_bf16 v[64:67], a[100:103], v[172:175], v[64:67]\n"
    "v_exp_f32 v153, v153\n"
    "v_mfma_f32_16x16x32_bf16 v[68:71], a[100:103], v[176:179], v[68:71]\n"
    "ds_read_b64_tr_b16 a[92:93], v199 offset:57344\n"
    "v_cvt_pk_bf16_f32 v182, v148, v149\n"
    "v_mfma_f32_16x16x32_bf16 v[72:75], a[104:107], v[168:171], v[72:75]\n"
    "v_exp_f32 v154, v154\n"
    "v_mfma_f32_16x16x32_bf16 v[76:79], a[104:107], v[172:175], v[76:79]\n"
    "ds_read_b64_tr_b16 a[94:95], v199 offset:61440\n"
    "v_cvt_pk_bf16_f32 v183, v150, v151\n"
    "v_mfma_f32_16x16x32_bf16 v[80:83], a[104:107], v[176:179], v[80:83]\n"
    "v_exp_f32 v155, v155\n"
    "v_mfma_f32_16x16x32_bf16 v[84:87], a[108:111], v[168:171], v[84:87]\n"
    "buffer_load_dwordx4 v209, s[48:51], s59 offen offset:1024 nt lds\n"
    "v_mfma_f32_16x16x32_bf16 v[88:91], a[108:111], v[172:175], v[88:91]\n"
    "s_add_u32 m0, s61, 24576\n"
    "v_exp_f32 v156, v156\n"
    "v_mfma_f32_16x16x32_bf16 v[92:95], a[108:111], v[176:179], v[92:95]\n"
    "ds_read_b64_tr_b16 a[96:97], v200 offset:57344\n"
    "v_cvt_pk_bf16_f32 v184, v152, v153\n"
    "s_waitcnt lgkmcnt(4)\n"
    "v_mfma_f32_16x16x32_bf16 v[120:123], a[48:51], a[0:3], v[108:111]\n"
    "v_exp_f32 v157, v157\n"
    "v_mfma_f32_16x16x32_bf16 v[120:123], a[52:55], a[4:7], v[120:123]\n"
    "ds_read_b64_tr_b16 a[98:99], v200 offset:61440\n"
    "v_cvt_pk_bf16_f32 v185, v154, v155\n"
    "v_mfma_f32_16x16x32_bf16 v[120:123], a[56:59], a[8:11], v[120:123]\n"
    "v_exp_f32 v158, v158\n"
    "v_mfma_f32_16x16x32_bf16 v[120:123], a[60:63], a[12:15], v[120:123]\n"
    "ds_read_b64_tr_b16 a[100:101], v201 offset:57344\n"
    "v_cvt_pk_bf16_f32 v186, v156, v157\n"
    "v_mfma_f32_16x16x32_bf16 v[124:127], a[64:67], a[0:3], v[108:111]\n"
    "v_exp_f32 v159, v159\n"
    "v_mfma_f32_16x16x32_bf16 v[124:127], a[68:71], a[4:7], v[124:127]\n"
    "ds_read_b64_tr_b16 a[102:103], v201 offset:61440\n"
    "v_cvt_pk_bf16_f32 v187, v158, v159\n"
    "v_mfma_f32_16x16x32_bf16 v[124:127], a[72:75], a[8:11], v[124:127]\n"
    "v_exp_f32 v160, v160\n"
    "v_mfma_f32_16x16x32_bf16 v[124:127], a[76:79], a[12:15], v[124:127]\n"
    "buffer_load_dwordx4 v204, s[40:43], s56 offen offset:0 nt lds\n"
    "v_mfma_f32_16x16x32_bf16 v[128:131], a[48:51], a[16:19], v[112:115]\n"
    "v_exp_f32 v161, v161\n"
    "v_mfma_f32_16x16x32_bf16 v[128:131], a[52:55], a[20:23], v[128:131]\n"
    "ds_read_b64_tr_b16 a[104:105], v202 offset:57344\n"
    "v_cvt_pk_bf16_f32 v188, v160, v161\n"
    "v_mfma_f32_16x16x32_bf16 v[128:131], a[56:59], a[24:27], v[128:131]\n"
    "v_exp_f32 v162, v162\n"
    "v_mfma_f32_16x16x32_bf16 v[128:131], a[60:63], a[28:31], v[128:131]\n"
    "ds_read_b64_tr_b16 a[106:107], v202 offset:61440\n"
    "ds_read_b64_tr_b16 a[108:109], v203 offset:57344\n"
    "v_mfma_f32_16x16x32_bf16 v[132:135], a[64:67], a[16:19], v[112:115]\n"
    "v_exp_f32 v163, v163\n"
    "v_mfma_f32_16x16x32_bf16 v[132:135], a[68:71], a[20:23], v[132:135]\n"
    "ds_read_b64_tr_b16 a[110:111], v203 offset:61440\n"
    "v_cvt_pk_bf16_f32 v189, v162, v163\n"
    "v_mfma_f32_16x16x32_bf16 v[132:135], a[72:75], a[24:27], v[132:135]\n"
    "v_exp_f32 v164, v164\n"
    "v_mfma_f32_16x16x32_bf16 v[132:135], a[76:79], a[28:31], v[132:135]\n"
    "ds_read_b128 a[124:127], v220 offset:2048\n"
    "ds_read_b128 a[128:131], v221 offset:3072\n"
    "v_mfma_f32_16x16x32_bf16 v[136:139], a[48:51], a[32:35], v[116:119]\n"
    "v_exp_f32 v165, v165\n"
    "v_mfma_f32_16x16x32_bf16 v[136:139], a[52:55], a[36:39], v[136:139]\n"
    "buffer_load_dwordx4 v205, s[40:43], s56 offen offset:1024 nt lds\n"
    "v_mfma_f32_16x16x32_bf16 v[136:139], a[56:59], a[40:43], v[136:139]\n"
    "s_xor_b32 s56, s56, 122880\n"
    "v_exp_f32 v166, v166\n"
    "v_mfma_f32_16x16x32_bf16 v[136:139], a[60:63], a[44:47], v[136:139]\n"
    "s_memrealtime s[94:95]\n"
    "v_cvt_pk_bf16_f32 v190, v164, v165\n"
    "v_mfma_f32_16x16x32_bf16 v[140:143], a[64:67], a[32:35], v[116:119]\n"
    "v_exp_f32 v167, v167\n"
    "v_mfma_f32_16x16x32_bf16 v[140:143], a[68:71], a[36:39], v[140:143]\n"
    "v_cvt_pk_bf16_f32 v191, v166, v167\n"
    "v_mfma_f32_16x16x32_bf16 v[140:143], a[72:75], a[40:43], v[140:143]\n"
    "v_mfma_f32_16x16x32_bf16 v[140:143], a[76:79], a[44:47], v[140:143]\n"
    "s_waitcnt vmcnt(11) lgkmcnt(0)\n"
    "s_waitcnt vmcnt(10)\n"
    "v_mfma_f32_16x16x32_bf16 v[96:99], a[112:115], v[180:183], v[96:99]\n"
    "Lpc3_%=:\n"
    "s_sub_u32 s96, s94, s97\n"
    "s_cmp_lt_i32 s96, 0\n"
    "s_cbranch_scc1 Lsp3_%=\n"
    "s_barrier\n"
    "ds_read_b128 a[48:51], v192 offset:8192\n"
    "ds_read_b128 a[52:55], v193 offset:8192\n"
    "v_mfma_f32_16x16x32_bf16 v[100:103], a[112:115], v[184:187], v[100:103]\n"
    "s_add_u32 m0, s61, 57344\n"
    "ds_read_b128 v[224:227], v212 offset:0\n"
    "v_exp_f32 v120, v120\n"
    "v_mfma_f32_16x16x32_bf16 v[104:107], a[112:115], v[188:191], v[104:107]\n"
    "s_sub_u32 s96, s96, 60\n"
    "ds_read_b128 a[56:59], v194 offset:8192\n"
    "ds_read_b128 a[60:63], v195 offset:8192\n"
    "v_mfma_f32_16x16x32_bf16 v[0:3], a[80:83], v[180:183], v[0:3]\n"
    "s_max_i32 s96, s96, 0\n"
    "v_exp_f32 v121, v121\n"
    "v_mfma_f32_16x16x32_bf16 v[4:7], a[80:83], v[184:187], v[4:7]\n"
    "s_add_u32 s97, s97, s96\n"
    "buffer_load_dwordx4 v206, s[44:47], s57 offen offset:0 nt lds\n"
    "v_mfma_f32_16x16x32_bf16 v[8:11], a[80:83], v[188:191], v[8:11]\n"
    "s_bfe_u32 s93, s77, 0x80000\n"
    "v_exp_f32 v122, v122\n"
    "v_mfma_f32_16x16x32_bf16 v[12:15], a[84:87], v[180:183], v[12:15]\n"
    "s_add_u32 s97, s97, s93\n"
    "ds_read_b128 a[64:67], v192 offset:12288\n"
    "ds_read_b128 a[68:71], v193 offset:12288\n"
    "v_mfma_f32_16x16x32_bf16 v[16:19], a[84:87], v[184:187], v[16:19]\n"
    "v_exp_f32 v123, v123\n"
    "v_mfma_f32_16x16x32_bf16 v[20:23], a[84:87], v[188:191], v[20:23]\n"
    "ds_read_b128 a[72:75], v194 offset:12288\n"
    "ds_read_b128 a[76:79], v195 offset:12288\n"
    "v_mfma_f32_16x16x32_bf16 v[24:27], a[88:91], v[180:183], v[24:27]\n"
    "v_exp_f32 v124, v124\n"
    "s_waitcnt lgkmcnt(6)\n"
    "v_lshlrev_b32 v240, 16, v224\n"
    "v_mfma_f32_16x16x32_bf16 v[28:31], a[88:91], v[184:187], v[28:31]\n"
    "ds_read_b64_tr_b16 a[80:81], v196 offset:32768\n"
    "ds_read_b64_tr_b16 a[82:83], v196 offset:36864\n"
    "v_lshlrev_b32 v242, 16, v225\n"
    "v_mfma_f32_16x16x32_bf16 v[32:35], a[88:91], v[188:191], v[32:35]\n"
    "v_exp_f32 v125, v125\n"
    "v_mfma_f32_16x16x32_bf16 v[36:39], a[92:95], v[180:183], v[36:39]\n"
    "ds_read_b64_tr_b16 a[84:85], v197 offset:32768\n"
    "ds_read_b64_tr_b16 a[86:87], v197 offset:36864\n"
    "v_and_b32 v241, s74, v224\n"
    "v_mfma_f32_16x16x32_bf16 v[40:43], a[92:95], v[184:187], v[40:43]\n"
    "v_exp_f32 v126, v126\n"
    "v_mfma_f32_16x16x32_bf16 v[44:47], a[92:95], v[188:191], v[44:47]\n"
    "buffer_load_dwordx4 v207, s[44:47], s57 offen offset:1024 nt lds\n"
    "v_and_b32 v243, s74, v225\n"
    "v_mfma_f32_16x16x32_bf16 v[48:51], a[96:99], v[180:183], v[48:51]\n"
    "s_xor_b32 s57, s57, 122880\n"
    "v_exp_f32 v127, v127\n"
    "v_mfma_f32_16x16x32_bf16 v[52:55], a[96:99], v[184:187], v[52:55]\n"
    "s_add_u32 m0, s62, 4096\n"
    "ds_read_b64_tr_b16 a[88:89], v198 offset:32768\n"
    "v_cvt_pk_bf16_f32 v168, v120, v121\n"
    "v_mul_f32 v240, s65, v240\n"
    "v_mfma_f32_16x16x32_bf16 v[56:59], a[96:99], v[188:191], v[56:59]\n"
    "v_exp_f32 v128, v128\n"
    "v_mul_f32 v242, s65, v242\n"
    "v_mfma_f32_16x16x32_bf16 v[60:63], a[100:103], v[180:183], v[60:63]\n"
    "ds_read_b64_tr_b16 a[90:91], v198 offset:36864\n"
    "v_cvt_pk_bf16_f32 v169, v122, v123\n"
    "v_mfma_f32_16x16x32_bf16 v[64:67], a[100:103], v[184:187], v[64:67]\n"
    "v_exp_f32 v129, v129\n"
    "v_mul_f32 v241, s65, v241\n"
    "v_mfma_f32_16x16x32_bf16 v[68:71], a[100:103], v[188:191], v[68:71]\n"
    "ds_read_b64_tr_b16 a[92:93], v199 offset:32768\n"
    "v_cvt_pk_bf16_f32 v170, v124, v125\n"
    "v_mfma_f32_16x16x32_bf16 v[72:75], a[104:107], v[180:183], v[72:75]\n"
    "v_exp_f32 v130, v130\n"
    "v_mul_f32 v243, s65, v243\n"
    "v_mfma_f32_16x16x32_bf16 v[76:79], a[104:107], v[184:187], v[76:79]\n"
    "ds_read_b64_tr_b16 a[94:95], v199 offset:36864\n"
    "v_cvt_pk_bf16_f32 v171, v126, v127\n"
    "v_mfma_f32_16x16x32_bf16 v[80:83], a[104:107], v[188:191], v[80:83]\n"
    "v_exp_f32 v131, v131\n"
    "v_cvt_pk_bf16_f32 v224, v240, v241\n"
    "v_mfma_f32_16x16x32_bf16 v[84:87], a[108:111], v[180:183], v[84:87]\n"
    "buffer_load_dwordx4 v210, s[48:51], s59 offen offset:2048 nt lds\n"
    "v_cvt_pk_bf16_f32 v225, v242, v243\n"
    "v_mfma_f32_16x16x32_bf16 v[88:91], a[108:111], v[184:187], v[88:91]\n"
    "s_add_u32 m0, s61, 0\n"
    "v_exp_f32 v132, v132\n"
    "v_mfma_f32_16x16x32_bf16 v[92:95], a[108:111], v[188:191], v[92:95]\n"
    "ds_read_b64_tr_b16 a[96:97], v200 offset:32768\n"
    "v_cvt_pk_bf16_f32 v172, v128, v129\n"
    "v_lshlrev_b32 v244, 16, v226\n"
    "v_mfma_f32_16x16x32_bf16 v[144:147], a[48:51], a[0:3], v[108:111]\n"
    "v_exp_f32 v133, v133\n"
    "v_mfma_f32_16x16x32_bf16 v[144:147], a[52:55], a[4:7], v[144:147]\n"
    "ds_read_b64_tr_b16 a[98:99], v200 offset:36864\n"
    "v_cvt_pk_bf16_f32 v173, v130, v131\n"
    "v_lshlrev_b32 v246, 16, v227\n"
    "s_waitcnt lgkmcnt(3)\n"
    "v_mfma_f32_16x16x32_bf16 v[144:147], a[56:59], a[8:11], v[144:147]\n"
    "v_exp_f32 v134, v134\n"
    "v_mfma_f32_16x16x32_bf16 v[144:147], a[60:63], a[12:15], v[144:147]\n"
    "ds_read_b64_tr_b16 a[100:101], v201 offset:32768\n"
    "v_cvt_pk_bf16_f32 v174, v132, v133\n"
    "v_and_b32 v245, s74, v226\n"
    "v_mfma_f32_16x16x32_bf16 v[148:151], a[64:67], a[0:3], v[108:111]\n"
    "v_exp_f32 v135, v135\n"
    "v_and_b32 v247, s74, v227\n"
    "v_mfma_f32_16x16x32_bf16 v[148:151], a[68:71], a[4:7], v[148:151]\n"
    "ds_read_b64_tr_b16 a[102:103], v201 offset:36864\n"
    "v_cvt_pk_bf16_f32 v175, v134, v135\n"
    "v_mfma_f32_16x16x32_bf16 v[148:151], a[72:75], a[8:11], v[148:151]\n"
    "v_exp_f32 v136, v136\n"
    "v_mul_f32 v244, s65, v244\n"
    "v_mfma_f32_16x16x32_bf16 v[148:151], a[76:79], a[12:15], v[148:151]\n"
    "buffer_load_dwordx4 v204, s[40:43], s56 offen offset:0 nt lds\n"
    "v_mfma_f32_16x16x32_bf16 v[152:155], a[48:51], a[16:19], v[112:115]\n"
    "v_exp_f32 v137, v137\n"
    "v_mul_f32 v246, s65, v246\n"
    "v_mfma_f32_16x16x32_bf16 v[152:155], a[52:55], a[20:23], v[152:155]\n"
    "ds_read_b64_tr_b16 a[104:105], v202 offset:32768\n"
    "v_cvt_pk_bf16_f32 v176, v136, v137\n"
    "v_mfma_f32_16x16x32_bf16 v[152:155], a[56:59], a[24:27], v[152:155]\n"
    "v_exp_f32 v138, v138\n"
    "v_mul_f32 v245, s65, v245\n"
    "v_mfma_f32_16x16x32_bf16 v[152:155], a[60:63], a[28:31], v[152:155]\n"
    "ds_read_b64_tr_b16 a[106:107], v202 offset:36864\n"
    "ds_read_b64_tr_b16 a[108:109], v203 offset:32768\n"
    "v_mul_f32 v247, s65, v247\n"
    "v_mfma_f32_16x16x32_bf16 v[156:159], a[64:67], a[16:19], v[112:115]\n"
    "v_exp_f32 v139, v139\n"
    "v_mfma_f32_16x16x32_bf16 v[156:159], a[68:71], a[20:23], v[156:159]\n"
    "ds_read_b64_tr_b16 a[110:111], v203 offset:36864\n"
    "v_cvt_pk_bf16_f32 v177, v138, v139\n"
    "v_cvt_pk_bf16_f32 v226, v244, v245\n"
    "v_mfma_f32_16x16x32_bf16 v[156:159], a[72:75], a[24:27], v[156:159]\n"
    "v_exp_f32 v140, v140\n"
    "v_mfma_f32_16x16x32_bf16 v[156:159], a[76:79], a[28:31], v[156:159]\n"
    "ds_read_b128 a[132:135], v220 offset:4096\n"
    "ds_read_b128 a[136:139], v221 offset:5120\n"
    "v_cvt_pk_bf16_f32 v227, v246, v247\n"
    "v_mfma_f32_16x16x32_bf16 v[160:163], a[48:51], a[32:35], v[116:119]\n"
    "v_exp_f32 v141, v141\n"
    "v_mfma_f32_16x16x32_bf16 v[160:163], a[52:55], a[36:39], v[160:163]\n"
    "buffer_load_dwordx4 v205, s[40:43], s56 offen offset:1024 nt lds\n"
    "v_mfma_f32_16x16x32_bf16 v[160:163], a[56:59], a[40:43], v[160:163]\n"
    "s_xor_b32 s56, s56, 8192\n"
    "ds_write_b128 v212, v[224:227] offset:0\n"
    "v_exp_f32 v142, v142\n"
    "v_mfma_f32_16x16x32_bf16 v[160:163], a[60:63], a[44:47], v[160:163]\n"
    "s_memrealtime s[94:95]\n"
    "v_cvt_pk_bf16_f32 v178, v140, v141\n"
    "v_mfma_f32_16x16x32_bf16 v[164:167], a[64:67], a[32:35], v[116:119]\n"
    "v_exp_f32 v143, v143\n"
    "v_mfma_f32_16x16x32_bf16 v[164:167], a[68:71], a[36:39], v[164:167]\n"
    "v_cvt_pk_bf16_f32 v179, v142, v143\n"
    "v_mfma_f32_16x16x32_bf16 v[164:167], a[72:75], a[40:43], v[164:167]\n"
    "v_mfma_f32_16x16x32_bf16 v[164:167], a[76:79], a[44:47], v[164:167]\n"
    "s_waitcnt vmcnt(10) lgkmcnt(0)\n"
    "v_mfma_f32_16x16x32_bf16 v[96:99], a[112:115], v[168:171], v[96:99]\n"
    "Lpc4_%=:\n"
    "s_sub_u32 s96, s94, s97\n"
    "s_cmp_lt_i32 s96, 0\n"
    "s_cbranch_scc1 Lsp4_%=\n"
    "s_barrier\n"
    "ds_read_b128 a[48:51], v192 offset:16384\n"
    "ds_read_b128 a[52:55], v193 offset:16384\n"
    "v_mfma_f32_16x16x32_bf16 v[100:103], a[112:115], v[172:175], v[100:103]\n"
    "s_add_u32 m0, s61, 32768\n"
    "ds_read_b128 v[228:231], v213 offset:0\n"
    "v_exp_f32 v144, v144\n"
    "v_mfma_f32_16x16x32_bf16 v[104:107], a[112:115], v[176:179], v[104:107]\n"
    "s_sub_u32 s96, s96, 60\n"
    "ds_read_b128 a[56:59], v194 offset:16384\n"
    "ds_read_b128 a[60:63], v195 offset:16384\n"
    "v_mfma_f32_16x16x32_bf16 v[0:3], a[80:83], v[168:171], v[0:3]\n"
    "s_max_i32 s96, s96, 0\n"
    "v_exp_f32 v145, v145\n"
    "v_mfma_f32_16x16x32_bf16 v[4:7], a[80:83], v[172:175], v[4:7]\n"
    "s_add_u32 s97, s97, s96\n"
    "buffer_load_dwordx4 v206, s[44:47], s57 offen offset:0 nt lds\n"
    "v_mfma_f32_16x16x32_bf16 v[8:11], a[80:83], v[176:179], v[8:11]\n"
    "s_bfe_u32 s93, s77, 0x80008\n"
    "v_exp_f32 v146, v146\n"
    "v_mfma_f32_16x16x32_bf16 v[12:15], a[84:87], v[168:171], v[12:15]\n"
    "s_add_u32 s97, s97, s93\n"
    "ds_read_b128 a[64:67], v192 offset:20480\n"
    "ds_read_b128 a[68:71], v193 offset:20480\n"
    "v_mfma_f32_16x16x32_bf16 v[16:19], a[84:87], v[172:175], v[16:19]\n"
    "v_exp_f32 v147, v147\n"
    "v_mfma_f32_16x16x32_bf16 v[20:23], a[84:87], v[176:179], v[20:23]\n"
    "ds_read_b128 a[72:75], v194 offset:20480\n"
    "ds_read_b128 a[76:79], v195 offset:20480\n"
    "v_mfma_f32_16x16x32_bf16 v[24:27], a[88:91], v[168:171], v[24:27]\n"
    "v_exp_f32 v148, v148\n"
    "s_waitcnt lgkmcnt(6)\n"
    "v_lshlrev_b32 v240, 16, v228\n"
    "v_mfma_f32_16x16x32_bf16 v[28:31], a[88:91], v[172:175], v[28:31]\n"
    "ds_read_b64_tr_b16 a[80:81], v196 offset:40960\n"
    "ds_read_b64_tr_b16 a[82:83], v196 offset:45056\n"
    "v_lshlrev_b32 v242, 16, v229\n"
    "v_mfma_f32_16x16x32_bf16 v[32:35], a[88:91], v[176:179], v[32:35]\n"
    "v_exp_f32 v149, v149\n"
    "v_mfma_f32_16x16x32_bf16 v[36:39], a[92:95], v[168:171], v[36:39]\n"
    "ds_read_b64_tr_b16 a[84:85], v197 offset:40960\n"
    "ds_read_b64_tr_b16 a[86:87], v197 offset:45056\n"
    "v_and_b32 v241, s74, v228\n"
    "v_mfma_f32_16x16x32_bf16 v[40:43], a[92:95], v[172:175], v[40:43]\n"
    "v_exp_f32 v150, v150\n"
    "v_mfma_f32_16x16x32_bf16 v[44:47], a[92:95], v[176:179], v[44:47]\n"
    "buffer_load_dwordx4 v207, s[44:47], s57 offen offset:1024 nt lds\n"
    "v_and_b32 v243, s74, v229\n"
    "v_mfma_f32_16x16x32_bf16 v[48:51], a[96:99], v[168:171], v[48:51]\n"
    "s_xor_b32 s57, s57, 8192\n"
    "v_exp_f32 v151, v151\n"
    "v_mfma_f32_16x16x32_bf16 v[52:55], a[96:99], v[172:175], v[52:55]\n"
    "s_add_u32 m0, s62, 4096\n"
    "ds_read_b64_tr_b16 a[88:89], v198 offset:40960\n"
    "v_cvt_pk_bf16_f32 v180, v144, v145\n"
    "v_mul_f32 v240, s65, v240\n"
    "v_mfma_f32_16x16x32_bf16 v[56:59], a[96:99], v[176:179], v[56:59]\n"
    "v_exp_f32 v152, v152\n"
    "v_mul_f32 v242, s65, v242\n"
    "v_mfma_f32_16x16x32_bf16 v[60:63], a[100:103], v[168:171], v[60:63]\n"
    "ds_read_b64_tr_b16 a[90:91], v198 offset:45056\n"
    "v_cvt_pk_bf16_f32 v181, v146, v147\n"
    "v_mfma_f32_16x16x32_bf16 v[64:67], a[100:103], v[172:175], v[64:67]\n"
    "v_exp_f32 v153, v153\n"
    "v_mul_f32 v241, s65, v241\n"
    "v_mfma_f32_16x16x32_bf16 v[68:71], a[100:103], v[176:179], v[68:71]\n"
    "ds_read_b64_tr_b16 a[92:93], v199 offset:40960\n"
    "v_cvt_pk_bf16_f32 v182, v148, v149\n"
    "v_mfma_f32_16x16x32_bf16 v[72:75], a[104:107], v[168:171], v[72:75]\n"
    "v_exp_f32 v154, v154\n"
    "v_mul_f32 v243, s65, v243\n"
    "v_mfma_f32_16x16x32_bf16 v[76:79], a[104:107], v[172:175], v[76:79]\n"
    "ds_read_b64_tr_b16 a[94:95], v199 offset:45056\n"
    "v_cvt_pk_bf16_f32 v183, v150, v151\n"
    "v_mfma_f32_16x16x32_bf16 v[80:83], a[104:107], v[176:179], v[80:83]\n"
    "v_exp_f32 v155, v155\n"
    "v_cvt_pk_bf16_f32 v228, v240, v241\n"
    "v_mfma_f32_16x16x32_bf16 v[84:87], a[108:111], v[168:171], v[84:87]\n"
    "buffer_load_dwordx4 v211, s[48:51], s59 offen offset:3072 nt lds\n"
    "v_cvt_pk_bf16_f32 v229, v242, v243\n"
    "v_mfma_f32_16x16x32_bf16 v[88:91], a[108:111], v[172:175], v[88:91]\n"
    "s_add_u32 m0, s61, 8192\n"
    "v_exp_f32 v156, v156\n"
    "v_mfma_f32_16x16x32_bf16 v[92:95], a[108:111], v[176:179], v[92:95]\n"
    "ds_read_b64_tr_b16 a[96:97], v200 offset:40960\n"
    "v_cvt_pk_bf16_f32 v184, v152, v153\n"
    "v_lshlrev_b32 v244, 16, v230\n"
    "v_mfma_f32_16x16x32_bf16 v[120:123], a[48:51], a[0:3], v[108:111]\n"
    "v_exp_f32 v157, v157\n"
    "v_mfma_f32_16x16x32_bf16 v[120:123], a[52:55], a[4:7], v[120:123]\n"
    "ds_read_b64_tr_b16 a[98:99], v200 offset:45056\n"
    "v_cvt_pk_bf16_f32 v185, v154, v155\n"
    "v_lshlrev_b32 v246, 16, v231\n"
    "s_waitcnt lgkmcnt(3)\n"
    "v_mfma_f32_16x16x32_bf16 v[120:123], a[56:59], a[8:11], v[120:123]\n"
    "v_exp_f32 v158, v158\n"
    "v_mfma_f32_16x16x32_bf16 v[120:123], a[60:63], a[12:15], v[120:123]\n"
    "ds_read_b64_tr_b16 a[100:101], v201 offset:40960\n"
    "v_cvt_pk_bf16_f32 v186, v156, v157\n"
    "v_and_b32 v245, s74, v230\n"
    "v_mfma_f32_16x16x32_bf16 v[124:127], a[64:67], a[0:3], v[108:111]\n"
    "v_exp_f32 v159, v159\n"
    "v_and_b32 v247, s74, v231\n"
    "v_mfma_f32_16x16x32_bf16 v[124:127], a[68:71], a[4:7], v[124:127]\n"
    "ds_read_b64_tr_b16 a[102:103], v201 offset:45056\n"
    "v_cvt_pk_bf16_f32 v187, v158, v159\n"
    "v_mfma_f32_16x16x32_bf16 v[124:127], a[72:75], a[8:11], v[124:127]\n"
    "v_exp_f32 v160, v160\n"
    "v_mul_f32 v244, s65, v244\n"
    "v_mfma_f32_16x16x32_bf16 v[124:127], a[76:79], a[12:15], v[124:127]\n"
    "buffer_load_dwordx4 v204, s[40:43], s56 offen offset:0 nt lds\n"
    "v_mfma_f32_16x16x32_bf16 v[128:131], a[48:51], a[16:19], v[112:115]\n"
    "v_exp_f32 v161, v161\n"
    "v_mul_f32 v246, s65, v246\n"
    "v_mfma_f32_16x16x32_bf16 v[128:131], a[52:55], a[20:23], v[128:131]\n"
    "ds_read_b64_tr_b16 a[104:105], v202 offset:40960\n"
    "v_cvt_pk_bf16_f32 v188, v160, v161\n"
    "v_mfma_f32_16x16x32_bf16 v[128:131], a[56:59], a[24:27], v[128:131]\n"
    "v_exp_f32 v162, v162\n"
    "v_mul_f32 v245, s65, v245\n"
    "v_mfma_f32_16x16x32_bf16 v[128:131], a[60:63], a[28:31], v[128:131]\n"
    "ds_read_b64_tr_b16 a[106:107], v202 offset:45056\n"
    "ds_read_b64_tr_b16 a[108:109], v203 offset:40960\n"
    "v_mul_f32 v247, s65, v247\n"
    "v_mfma_f32_16x16x32_bf16 v[132:135], a[64:67], a[16:19], v[112:115]\n"
    "v_exp_f32 v163, v163\n"
    "v_mfma_f32_16x16x32_bf16 v[132:135], a[68:71], a[20:23], v[132:135]\n"
    "ds_read_b64_tr_b16 a[110:111], v203 offset:45056\n"
    "v_cvt_pk_bf16_f32 v189, v162, v163\n"
    "v_cvt_pk_bf16_f32 v230, v244, v245\n"
    "v_mfma_f32_16x16x32_bf16 v[132:135], a[72:75], a[24:27], v[132:135]\n"
    "v_exp_f32 v164, v164\n"
    "v_mfma_f32_16x16x32_bf16 v[132:135], a[76:79], a[28:31], v[132:135]\n"
    "ds_read_b128 a[140:143], v220 offset:6144\n"
    "ds_read_b128 a[144:147], v221 offset:7168\n"
    "v_cvt_pk_bf16_f32 v231, v246, v247\n"
    "v_mfma_f32_16x16x32_bf16 v[136:139], a[48:51], a[32:35], v[116:119]\n"
    "v_exp_f32 v165, v165\n"
    "v_mfma_f32_16x16x32_bf16 v[136:139], a[52:55], a[36:39], v[136:139]\n"
    "buffer_load_dwordx4 v205, s[40:43], s56 offen offset:1024 nt lds\n"
    "v_mfma_f32_16x16x32_bf16 v[136:139], a[56:59], a[40:43], v[136:139]\n"
    "s_xor_b32 s56, s56, 24576\n"
    "ds_write_b128 v213, v[228:231] offset:0\n"
    "v_exp_f32 v166, v166\n"
    "v_mfma_f32_16x16x32_bf16 v[136:139], a[60:63], a[44:47], v[136:139]\n"
    "s_memrealtime s[94:95]\n"
    "v_cvt_pk_bf16_f32 v190, v164, v165\n"
    "v_mfma_f32_16x16x32_bf16 v[140:143], a[64:67], a[32:35], v[116:119]\n"
    "v_exp_f32 v167, v167\n"
    "v_mfma_f32_16x16x32_bf16 v[140:143], a[68:71], a[36:39], v[140:143]\n"
    "v_cvt_pk_bf16_f32 v191, v166, v167\n"
    "v_mfma_f32_16x16x32_bf16 v[140:143], a[72:75], a[40:43], v[140:143]\n"
    "v_mfma_f32_16x16x32_bf16 v[140:143], a[76:79], a[44:47], v[140:143]\n"
    "s_waitcnt vmcnt(10) lgkmcnt(0)\n"
    "v_mfma_f32_16x16x32_bf16 v[96:99], a[112:115], v[180:183], v[96:99]\n"
    "Lpc5_%=:\n"
    "s_sub_u32 s96, s94, s97\n"
    "s_cmp_lt_i32 s96, 0\n"
    "s_cbranch_scc1 Lsp5_%=\n"
    "s_barrier\n"
    "ds_read_b128 a[48:51], v192 offset:24576\n"
    "ds_read_b128 a[52:55], v193 offset:24576\n"
    "v_mfma_f32_16x16x32_bf16 v[100:103], a[112:115], v[184:187], v[100:103]\n"
    "s_add_u32 m0, s61, 40960\n"
    "ds_read_b128 v[232:235], v214 offset:0\n"
    "v_exp_f32 v120, v120\n"
    "v_mfma_f32_16x16x32_bf16 v[104:107], a[112:115], v[188:191], v[104:107]\n"
    "s_sub_u32 s96, s96, 60\n"
    "ds_read_b128 a[56:59], v194 offset:24576\n"
    "ds_read_b128 a[60:63], v195 offset:24576\n"
    "v_mfma_f32_16x16x32_bf16 v[0:3], a[80:83], v[180:183], v[0:3]\n"
    "s_max_i32 s96, s96, 0\n"
    "v_exp_f32 v121, v121\n"
    "v_mfma_f32_16x16x32_bf16 v[4:7], a[80:83], v[184:187], v[4:7]\n"
    "s_add_u32 s97, s97, s96\n"
    "buffer_load_dwordx4 v206, s[44:47], s57 offen offset:0 nt lds\n"
    "v_mfma_f32_16x16x32_bf16 v[8:11], a[80:83], v[188:191], v[8:11]\n"
    "s_bfe_u32 s93, s77, 0x80010\n"
    "v_exp_f32 v122, v122\n"
    "v_mfma_f32_16x16x32_bf16 v[12:15], a[84:87], v[180:183], v[12:15]\n"
    "s_add_u32 s97, s97, s93\n"
    "ds_read_b128 a[64:67], v192 offset:28672\n"
    "ds_read_b128 a[68:71], v193 offset:28672\n"
    "v_mfma_f32_16x16x32_bf16 v[16:19], a[84:87], v[184:187], v[16:19]\n"
    "v_exp_f32 v123, v123\n"
    "v_mfma_f32_16x16x32_bf16 v[20:23], a[84:87], v[188:191], v[20:23]\n"
    "ds_read_b128 a[72:75], v194 offset:28672\n"
    "ds_read_b128 a[76:79], v195 offset:28672\n"
    "v_mfma_f32_16x16x32_bf16 v[24:27], a[88:91], v[180:183], v[24:27]\n"
    "v_exp_f32 v124, v124\n"
    "s_waitcnt lgkmcnt(6)\n"
    "v_lshlrev_b32 v240, 16, v232\n"
    "v_mfma_f32_16x16x32_bf16 v[28:31], a[88:91], v[184:187], v[28:31]\n"
    "ds_read_b64_tr_b16 a[80:81], v196 offset:49152\n"
    "ds_read_b64_tr_b16 a[82:83], v196 offset:53248\n"
    "v_lshlrev_b32 v242, 16, v233\n"
    "v_mfma_f32_16x16x32_bf16 v[32:35], a[88:91], v[188:191], v[32:35]\n"
    "v_exp_f32 v125, v125\n"
    "v_mfma_f32_16x16x32_bf16 v[36:39], a[92:95], v[180:183], v[36:39]\n"
    "ds_read_b64_tr_b16 a[84:85], v197 offset:49152\n"
    "ds_read_b64_tr_b16 a[86:87], v197 offset:53248\n"
    "v_and_b32 v241, s74, v232\n"
    "v_mfma_f32_16x16x32_bf16 v[40:43], a[92:95], v[184:187], v[40:43]\n"
    "v_exp_f32 v126, v126\n"
    "v_mfma_f32_16x16x32_bf16 v[44:47], a[92:95], v[188:191], v[44:47]\n"
    "buffer_load_dwordx4 v207, s[44:47], s57 offen offset:1024 nt lds\n"
    "v_and_b32 v243, s74, v233\n"
    "v_mfma_f32_16x16x32_bf16 v[48:51], a[96:99], v[180:183], v[48:51]\n"
    "s_xor_b32 s57, s57, 24576\n"
    "v_exp_f32 v127, v127\n"
    "v_mfma_f32_16x16x32_bf16 v[52:55], a[96:99], v[184:187], v[52:55]\n"
    "s_add_u32 m0, s62, 8192\n"
    "ds_read_b64_tr_b16 a[88:89], v198 offset:49152\n"
    "v_cvt_pk_bf16_f32 v168, v120, v121\n"
    "v_mul_f32 v240, s65, v240\n"
    "v_mfma_f32_16x16x32_bf16 v[56:59], a[96:99], v[188:191], v[56:59]\n"
    "v_exp_f32 v128, v128\n"
    "v_mul_f32 v242, s65, v242\n"
    "v_mfma_f32_16x16x32_bf16 v[60:63], a[100:103], v[180:183], v[60:63]\n"
    "ds_read_b64_tr_b16 a[90:91], v198 offset:53248\n"
    "v_cvt_pk_bf16_f32 v169, v122, v123\n"
    "v_mfma_f32_16x16x32_bf16 v[64:67], a[100:103], v[184:187], v[64:67]\n"
    "v_exp_f32 v129, v129\n"
    "v_mul_f32 v241, s65, v241\n"
    "v_mfma_f32_16x16x32_bf16 v[68:71], a[100:103], v[188:191], v[68:71]\n"
    "ds_read_b64_tr_b16 a[92:93], v199 offset:49152\n"
    "v_cvt_pk_bf16_f32 v170, v124, v125\n"
    "v_mfma_f32_16x16x32_bf16 v[72:75], a[104:107], v[180:183], v[72:75]\n"
    "v_exp_f32 v130, v130\n"
    "v_mul_f32 v243, s65, v243\n"
    "v_mfma_f32_16x16x32_bf16 v[76:79], a[104:107], v[184:187], v[76:79]\n"
    "ds_read_b64_tr_b16 a[94:95], v199 offset:53248\n"
    "v_cvt_pk_bf16_f32 v171, v126, v127\n"
    "v_mfma_f32_16x16x32_bf16 v[80:83], a[104:107], v[188:191], v[80:83]\n"
    "v_exp_f32 v131, v131\n"
    "v_cvt_pk_bf16_f32 v232, v240, v241\n"
    "v_mfma_f32_16x16x32_bf16 v[84:87], a[108:111], v[180:183], v[84:87]\n"
    "buffer_load_dwordx4 v208, s[48:51], s60 offen offset:0 nt lds\n"
    "v_cvt_pk_bf16_f32 v233, v242, v243\n"
    "v_mfma_f32_16x16x32_bf16 v[88:91], a[108:111], v[184:187], v[88:91]\n"
    "s_add_u32 m0, s61, 16384\n"
    "v_exp_f32 v132, v132\n"
    "v_mfma_f32_16x16x32_bf16 v[92:95], a[108:111], v[188:191], v[92:95]\n"
    "ds_read_b64_tr_b16 a[96:97], v200 offset:49152\n"
    "v_cvt_pk_bf16_f32 v172, v128, v129\n"
    "v_lshlrev_b32 v244, 16, v234\n"
    "v_mfma_f32_16x16x32_bf16 v[144:147], a[48:51], a[0:3], v[108:111]\n"
    "v_exp_f32 v133, v133\n"
    "v_mfma_f32_16x16x32_bf16 v[144:147], a[52:55], a[4:7], v[144:147]\n"
    "ds_read_b64_tr_b16 a[98:99], v200 offset:53248\n"
    "v_cvt_pk_bf16_f32 v173, v130, v131\n"
    "v_lshlrev_b32 v246, 16, v235\n"
    "s_waitcnt lgkmcnt(3)\n"
    "v_mfma_f32_16x16x32_bf16 v[144:147], a[56:59], a[8:11], v[144:147]\n"
    "v_exp_f32 v134, v134\n"
    "v_mfma_f32_16x16x32_bf16 v[144:147], a[60:63], a[12:15], v[144:147]\n"
    "ds_read_b64_tr_b16 a[100:101], v201 offset:49152\n"
    "v_cvt_pk_bf16_f32 v174, v132, v133\n"
    "v_and_b32 v245, s74, v234\n"
    "v_mfma_f32_16x16x32_bf16 v[148:151], a[64:67], a[0:3], v[108:111]\n"
    "v_exp_f32 v135, v135\n"
    "v_and_b32 v247, s74, v235\n"
    "v_mfma_f32_16x16x32_bf16 v[148:151], a[68:71], a[4:7], v[148:151]\n"
    "ds_read_b64_tr_b16 a[102:103], v201 offset:53248\n"
    "v_cvt_pk_bf16_f32 v175, v134, v135\n"
    "v_mfma_f32_16x16x32_bf16 v[148:151], a[72:75], a[8:11], v[148:151]\n"
    "v_exp_f32 v136, v136\n"
    "v_mul_f32 v244, s65, v244\n"
    "v_mfma_f32_16x16x32_bf16 v[148:151], a[76:79], a[12:15], v[148:151]\n"
    "buffer_load_dwordx4 v204, s[40:43], s56 offen offset:0 nt lds\n"
    "v_mfma_f32_16x16x32_bf16 v[152:155], a[48:51], a[16:19], v[112:115]\n"
    "v_exp_f32 v137, v137\n"
    "v_mul_f32 v246, s65, v246\n"
    "v_mfma_f32_16x16x32_bf16 v[152:155], a[52:55], a[20:23], v[152:155]\n"
    "ds_read_b64_tr_b16 a[104:105], v202 offset:49152\n"
    "v_cvt_pk_bf16_f32 v176, v136, v137\n"
    "v_mfma_f32_16x16x32_bf16 v[152:155], a[56:59], a[24:27], v[152:155]\n"
    "v_exp_f32 v138, v138\n"
    "v_mul_f32 v245, s65, v245\n"
    "v_mfma_f32_16x16x32_bf16 v[152:155], a[60:63], a[28:31], v[152:155]\n"
    "ds_read_b64_tr_b16 a[106:107], v202 offset:53248\n"
    "ds_read_b64_tr_b16 a[108:109], v203 offset:49152\n"
    "v_mul_f32 v247, s65, v247\n"
    "v_mfma_f32_16x16x32_bf16 v[156:159], a[64:67], a[16:19], v[112:115]\n"
    "v_exp_f32 v139, v139\n"
    "v_mfma_f32_16x16x32_bf16 v[156:159], a[68:71], a[20:23], v[156:159]\n"
    "ds_read_b64_tr_b16 a[110:111], v203 offset:53248\n"
    "v_cvt_pk_bf16_f32 v177, v138, v139\n"
    "v_cvt_pk_bf16_f32 v234, v244, v245\n"
    "v_mfma_f32_16x16x32_bf16 v[156:159], a[72:75], a[24:27], v[156:159]\n"
    "v_exp_f32 v140, v140\n"
    "v_mfma_f32_16x16x32_bf16 v[156:159], a[76:79], a[28:31], v[156:159]\n"
    "ds_read_b128 a[148:151], v220 offset:8192\n"
    "ds_read_b128 a[152:155], v221 offset:9216\n"
    "v_cvt_pk_bf16_f32 v235, v246, v247\n"
    "v_mfma_f32_16x16x32_bf16 v[160:163], a[48:51], a[32:35], v[116:119]\n"
    "v_exp_f32 v141, v141\n"
    "v_mfma_f32_16x16x32_bf16 v[160:163], a[52:55], a[36:39], v[160:163]\n"
    "buffer_load_dwordx4 v205, s[40:43], s56 offen offset:1024 nt lds\n"
    "v_mfma_f32_16x16x32_bf16 v[160:163], a[56:59], a[40:43], v[160:163]\n"
    "s_xor_b32 s56, s56, 8192\n"
    "ds_write_b128 v214, v[232:235] offset:0\n"
    "v_exp_f32 v142, v142\n"
    "v_mfma_f32_16x16x32_bf16 v[160:163], a[60:63], a[44:47], v[160:163]\n"
    "s_memrealtime s[94:95]\n"
    "v_cvt_pk_bf16_f32 v178, v140, v141\n"
    "v_mfma_f32_16x16x32_bf16 v[164:167], a[64:67], a[32:35], v[116:119]\n"
    "v_exp_f32 v143, v143\n"
    "v_mfma_f32_16x16x32_bf16 v[164:167], a[68:71], a[36:39], v[164:167]\n"
    "v_cvt_pk_bf16_f32 v179, v142, v143\n"
    "v_mfma_f32_16x16x32_bf16 v[164:167], a[72:75], a[40:43], v[164:167]\n"
    "v_mfma_f32_16x16x32_bf16 v[164:167], a[76:79], a[44:47], v[164:167]\n"
    "s_waitcnt vmcnt(10) lgkmcnt(0)\n"
    "v_mfma_f32_16x16x32_bf16 v[96:99], a[112:115], v[168:171], v[96:99]\n"
    "Lpc6_%=:\n"
    "s_sub_u32 s96, s94, s97\n"
    "s_cmp_lt_i32 s96, 0\n"
    "s_cbranch_scc1 Lsp6_%=\n"
    "s_barrier\n"
    "ds_read_b128 a[48:51], v192 offset:0\n"
    "ds_read_b128 a[52:55], v193 offset:0\n"
    "v_mfma_f32_16x16x32_bf16 v[100:103], a[112:115], v[172:175], v[100:103]\n"
    "s_add_u32 m0, s61, 49152\n"
    "ds_read_b128 v[236:239], v215 offset:0\n"
    "v_exp_f32 v144, v144\n"
    "v_mfma_f32_16x16x32_bf16 v[104:107], a[112:115], v[176:179], v[104:107]\n"
    "s_sub_u32 s96, s96, 60\n"
    "ds_read_b128 a[56:59], v194 offset:0\n"
    "ds_read_b128 a[60:63], v195 offset:0\n"
    "v_mfma_f32_16x16x32_bf16 v[0:3], a[80:83], v[168:171], v[0:3]\n"
    "s_max_i32 s96, s96, 0\n"
    "v_exp_f32 v145, v145\n"
    "v_mfma_f32_16x16x32_bf16 v[4:7], a[80:83], v[172:175], v[4:7]\n"
    "s_add_u32 s97, s97, s96\n"
    "buffer_load_dwordx4 v206, s[44:47], s57 offen offset:0 nt lds\n"
    "v_mfma_f32_16x16x32_bf16 v[8:11], a[80:83], v[176:179], v[8:11]\n"
    "s_bfe_u32 s93, s77, 0x80018\n"
    "v_exp_f32 v146, v146\n"
    "v_mfma_f32_16x16x32_bf16 v[12:15], a[84:87], v[168:171], v[12:15]\n"
    "s_add_u32 s97, s97, s93\n"
    "ds_read_b128 a[64:67], v192 offset:4096\n"
    "ds_read_b128 a[68:71], v193 offset:4096\n"
    "v_mfma_f32_16x16x32_bf16 v[16:19], a[84:87], v[172:175], v[16:19]\n"
    "v_exp_f32 v147, v147\n"
    "v_mfma_f32_16x16x32_bf16 v[20:23], a[84:87], v[176:179], v[20:23]\n"
    "ds_read_b128 a[72:75], v194 offset:4096\n"
    "ds_read_b128 a[76:79], v195 offset:4096\n"
    "v_mfma_f32_16x16x32_bf16 v[24:27], a[88:91], v[168:171], v[24:27]\n"
    "v_exp_f32 v148, v148\n"
    "s_waitcnt lgkmcnt(6)\n"
    "v_lshlrev_b32 v240, 16, v236\n"
    "v_mfma_f32_16x16x32_bf16 v[28:31], a[88:91], v[172:175], v[28:31]\n"
    "ds_read_b64_tr_b16 a[80:81], v196 offset:57344\n"
    "ds_read_b64_tr_b16 a[82:83], v196 offset:61440\n"
    "v_lshlrev_b32 v242, 16, v237\n"
    "v_mfma_f32_16x16x32_bf16 v[32:35], a[88:91], v[176:179], v[32:35]\n"
    "v_exp_f32 v149, v149\n"
    "v_mfma_f32_16x16x32_bf16 v[36:39], a[92:95], v[168:171], v[36:39]\n"
    "ds_read_b64_tr_b16 a[84:85], v197 offset:57344\n"
    "ds_read_b64_tr_b16 a[86:87], v197 offset:61440\n"
    "v_and_b32 v241, s74, v236\n"
    "v_mfma_f32_16x16x32_bf16 v[40:43], a[92:95], v[172:175], v[40:43]\n"
    "v_exp_f32 v150, v150\n"
    "v_mfma_f32_16x16x32_bf16 v[44:47], a[92:95], v[176:179], v[44:47]\n"
    "buffer_load_dwordx4 v207, s[44:47], s57 offen offset:1024 nt lds\n"
    "v_and_b32 v243, s74, v237\n"
    "v_mfma_f32_16x16x32_bf16 v[48:51], a[96:99], v[168:171], v[48:51]\n"
    "s_xor_b32 s57, s57, 8192\n"
    "v_exp_f32 v151, v151\n"
    "v_mfma_f32_16x16x32_bf16 v[52:55], a[96:99], v[172:175], v[52:55]\n"
    "s_add_u32 m0, s62, 8192\n"
    "ds_read_b64_tr_b16 a[88:89], v198 offset:57344\n"
    "v_cvt_pk_bf16_f32 v180, v144, v145\n"
    "v_mul_f32 v240, s65, v240\n"
    "v_mfma_f32_16x16x32_bf16 v[56:59], a[96:99], v[176:179], v[56:59]\n"
    "v_exp_f32 v152, v152\n"
    "v_mul_f32 v242, s65, v242\n"
    "v_mfma_f32_16x16x32_bf16 v[60:63], a[100:103], v[168:171], v[60:63]\n"
    "ds_read_b64_tr_b16 a[90:91], v198 offset:61440\n"
    "v_cvt_pk_bf16_f32 v181, v146, v147\n"
    "v_mfma_f32_16x16x32_bf16 v[64:67], a[100:103], v[172:175], v[64:67]\n"
    "v_exp_f32 v153, v153\n"
    "v_mul_f32 v241, s65, v241\n"
    "v_mfma_f32_16x16x32_bf16 v[68:71], a[100:103], v[176:179], v[68:71]\n"
    "ds_read_b64_tr_b16 a[92:93], v199 offset:57344\n"
    "v_cvt_pk_bf16_f32 v182, v148, v149\n"
    "v_mfma_f32_16x16x32_bf16 v[72:75], a[104:107], v[168:171], v[72:75]\n"
    "v_exp_f32 v154, v154\n"
    "v_mul_f32 v243, s65, v243\n"
    "v_mfma_f32_16x16x32_bf16 v[76:79], a[104:107], v[172:175], v[76:79]\n"
    "ds_read_b64_tr_b16 a[94:95], v199 offset:61440\n"
    "v_cvt_pk_bf16_f32 v183, v150, v151\n"
    "v_mfma_f32_16x16x32_bf16 v[80:83], a[104:107], v[176:179], v[80:83]\n"
    "v_exp_f32 v155, v155\n"
    "v_cvt_pk_bf16_f32 v236, v240, v241\n"
    "v_mfma_f32_16x16x32_bf16 v[84:87], a[108:111], v[168:171], v[84:87]\n"
    "buffer_load_dwordx4 v209, s[48:51], s60 offen offset:1024 nt lds\n"
    "v_cvt_pk_bf16_f32 v237, v242, v243\n"
    "v_mfma_f32_16x16x32_bf16 v[88:91], a[108:111], v[172:175], v[88:91]\n"
    "s_add_u32 m0, s61, 24576\n"
    "v_exp_f32 v156, v156\n"
    "v_mfma_f32_16x16x32_bf16 v[92:95], a[108:111], v[176:179], v[92:95]\n"
    "ds_read_b64_tr_b16 a[96:97], v200 offset:57344\n"
    "v_cvt_pk_bf16_f32 v184, v152, v153\n"
    "v_lshlrev_b32 v244, 16, v238\n"
    "v_mfma_f32_16x16x32_bf16 v[120:123], a[48:51], a[0:3], v[108:111]\n"
    "v_exp_f32 v157, v157\n"
    "v_mfma_f32_16x16x32_bf16 v[120:123], a[52:55], a[4:7], v[120:123]\n"
    "ds_read_b64_tr_b16 a[98:99], v200 offset:61440\n"
    "v_cvt_pk_bf16_f32 v185, v154, v155\n"
    "v_lshlrev_b32 v246, 16, v239\n"
    "s_waitcnt lgkmcnt(3)\n"
    "v_mfma_f32_16x16x32_bf16 v[120:123], a[56:59], a[8:11], v[120:123]\n"
    "v_exp_f32 v158, v158\n"
    "v_mfma_f32_16x16x32_bf16 v[120:123], a[60:63], a[12:15], v[120:123]\n"
    "ds_read_b64_tr_b16 a[100:101], v201 offset:57344\n"
    "v_cvt_pk_bf16_f32 v186, v156, v157\n"
    "v_and_b32 v245, s74, v238\n"
    "v_mfma_f32_16x16x32_bf16 v[124:127], a[64:67], a[0:3], v[108:111]\n"
    "v_exp_f32 v159, v159\n"
    "v_and_b32 v247, s74, v239\n"
    "v_mfma_f32_16x16x32_bf16 v[124:127], a[68:71], a[4:7], v[124:127]\n"
    "ds_read_b64_tr_b16 a[102:103], v201 offset:61440\n"
    "v_cvt_pk_bf16_f32 v187, v158, v159\n"
    "v_mfma_f32_16x16x32_bf16 v[124:127], a[72:75], a[8:11], v[124:127]\n"
    "v_exp_f32 v160, v160\n"
    "v_mul_f32 v244, s65, v244\n"
    "v_mfma_f32_16x16x32_bf16 v[124:127], a[76:79], a[12:15], v[124:127]\n"
    "buffer_load_dwordx4 v204, s[40:43], s56 offen offset:0 nt lds\n"
    "v_mfma_f32_16x16x32_bf16 v[128:131], a[48:51], a[16:19], v[112:115]\n"
    "v_exp_f32 v161, v161\n"
    "v_mul_f32 v246, s65, v246\n"
    "v_mfma_f32_16x16x32_bf16 v[128:131], a[52:55], a[20:23], v[128:131]\n"
    "ds_read_b64_tr_b16 a[104:105], v202 offset:57344\n"
    "v_cvt_pk_bf16_f32 v188, v160, v161\n"
    "v_mfma_f32_16x16x32_bf16 v[128:131], a[56:59], a[24:27], v[128:131]\n"
    "v_exp_f32 v162, v162\n"
    "v_mul_f32 v245, s65, v245\n"
    "v_mfma_f32_16x16x32_bf16 v[128:131], a[60:63], a[28:31], v[128:131]\n"
    "ds_read_b64_tr_b16 a[106:107], v202 offset:61440\n"
    "ds_read_b64_tr_b16 a[108:109], v203 offset:57344\n"
    "v_mul_f32 v247, s65, v247\n"
    "v_mfma_f32_16x16x32_bf16 v[132:135], a[64:67], a[16:19], v[112:115]\n"
    "v_exp_f32 v163, v163\n"
    "v_mfma_f32_16x16x32_bf16 v[132:135], a[68:71], a[20:23], v[132:135]\n"
    "ds_read_b64_tr_b16 a[110:111], v203 offset:61440\n"
    "v_cvt_pk_bf16_f32 v189, v162, v163\n"
    "v_cvt_pk_bf16_f32 v238, v244, v245\n"
    "v_mfma_f32_16x16x32_bf16 v[132:135], a[72:75], a[24:27], v[132:135]\n"
    "v_exp_f32 v164, v164\n"
    "v_mfma_f32_16x16x32_bf16 v[132:135], a[76:79], a[28:31], v[132:135]\n"
    "ds_read_b128 a[156:159], v220 offset:10240\n"
    "ds_read_b128 a[160:163], v221 offset:11264\n"
    "v_cvt_pk_bf16_f32 v239, v246, v247\n"
    "v_mfma_f32_16x16x32_bf16 v[136:139], a[48:51], a[32:35], v[116:119]\n"
    "v_exp_f32 v165, v165\n"
    "v_mfma_f32_16x16x32_bf16 v[136:139], a[52:55], a[36:39], v[136:139]\n"
    "buffer_load_dwordx4 v205, s[40:43], s56 offen offset:1024 nt lds\n"
    "v_mfma_f32_16x16x32_bf16 v[136:139], a[56:59], a[40:43], v[136:139]\n"
    "s_xor_b32 s56, s56, 57344\n"
    "ds_write_b128 v215, v[236:239] offset:0\n"
    "v_exp_f32 v166, v166\n"
    "v_mfma_f32_16x16x32_bf16 v[136:139], a[60:63], a[44:47], v[136:139]\n"
    "s_memrealtime s[94:95]\n"
    "v_cvt_pk_bf16_f32 v190, v164, v165\n"
    "v_mfma_f32_16x16x32_bf16 v[140:143], a[64:67], a[32:35], v[116:119]\n"
    "v_exp_f32 v167, v167\n"
    "v_mfma_f32_16x16x32_bf16 v[140:143], a[68:71], a[36:39], v[140:143]\n"
    "v_cvt_pk_bf16_f32 v191, v166, v167\n"
    "v_mfma_f32_16x16x32_bf16 v[140:143], a[72:75], a[40:43], v[140:143]\n"
    "v_mfma_f32_16x16x32_bf16 v[140:143], a[76:79], a[44:47], v[140:143]\n"
    "s_waitcnt vmcnt(10) lgkmcnt(0)\n"
    "s_waitcnt vmcnt(12)\n"
    "v_mfma_f32_16x16x32_bf16 v[96:99], a[112:115], v[180:183], v[96:99]\n"
    "Lpc7_%=:\n"
    "s_sub_u32 s96, s94, s97\n"
    "s_cmp_lt_i32 s96, 0\n"
    "s_cbranch_scc1 Lsp7_%=\n"
    "s_barrier\n"
    "ds_read_b128 a[48:51], v192 offset:8192\n"
    "ds_read_b128 a[52:55], v193 offset:8192\n"
    "v_mfma_f32_16x16x32_bf16 v[100:103], a[112:115], v[184:187], v[100:103]\n"
    "s_add_u32 m0, s61, 57344\n"
    "ds_read_b128 v[224:227], v212 offset:4096\n"
    "v_exp_f32 v120, v120\n"
    "v_mfma_f32_16x16x32_bf16 v[104:107], a[112:115], v[188:191], v[104:107]\n"
    "s_sub_u32 s96, s96, 60\n"
    "ds_read_b128 a[56:59], v194 offset:8192\n"
    "ds_read_b128 a[60:63], v195 offset:8192\n"
    "v_mfma_f32_16x16x32_bf16 v[0:3], a[80:83], v[180:183], v[0:3]\n"
    "s_max_i32 s96, s96, 0\n"
    "v_exp_f32 v121, v121\n"
    "v_mfma_f32_16x16x32_bf16 v[4:7], a[80:83], v[184:187], v[4:7]\n"
    "s_add_u32 s97, s97, s96\n"
    "buffer_load_dwordx4 v206, s[44:47], s57 offen offset:0 nt lds\n"
    "v_mfma_f32_16x16x32_bf16 v[8:11], a[80:83], v[188:191], v[8:11]\n"
    "s_bfe_u32 s93, s78, 0x80000\n"
    "v_exp_f32 v122, v122\n"
    "v_mfma_f32_16x16x32_bf16 v[12:15], a[84:87], v[180:183], v[12:15]\n"
    "s_add_u32 s97, s97, s93\n"
    "ds_read_b128 a[64:67], v192 offset:12288\n"
    "ds_read_b128 a[68:71], v193 offset:12288\n"
    "v_mfma_f32_16x16x32_bf16 v[16:19], a[84:87], v[184:187], v[16:19]\n"
    "v_exp_f32 v123, v123\n"
    "v_mfma_f32_16x16x32_bf16 v[20:23], a[84:87], v[188:191], v[20:23]\n"
    "ds_read_b128 a[72:75], v194 offset:12288\n"
    "ds_read_b128 a[76:79], v195 offset:12288\n"
    "v_mfma_f32_16x16x32_bf16 v[24:27], a[88:91], v[180:183], v[24:27]\n"
    "v_exp_f32 v124, v124\n"
    "s_waitcnt lgkmcnt(6)\n"
    "v_lshlrev_b32 v240, 16, v224\n"
    "v_mfma_f32_16x16x32_bf16 v[28:31], a[88:91], v[184:187], v[28:31]\n"
    "ds_read_b64_tr_b16 a[80:81], v196 offset:32768\n"
    "ds_read_b64_tr_b16 a[82:83], v196 offset:36864\n"
    "v_lshlrev_b32 v242, 16, v225\n"
    "v_mfma_f32_16x16x32_bf16 v[32:35], a[88:91], v[188:191], v[32:35]\n"
    "v_exp_f32 v125, v125\n"
    "v_mfma_f32_16x16x32_bf16 v[36:39], a[92:95], v[180:183], v[36:39]\n"
    "ds_read_b64_tr_b16 a[84:85], v197 offset:32768\n"
    "ds_read_b64_tr_b16 a[86:87], v197 offset:36864\n"
    "v_and_b32 v241, s74, v224\n"
    "v_mfma_f32_16x16x32_bf16 v[40:43], a[92:95], v[184:187], v[40:43]\n"
    "v_exp_f32 v126, v126\n"
    "v_mfma_f32_16x16x32_bf16 v[44:47], a[92:95], v[188:191], v[44:47]\n"
    "buffer_load_dwordx4 v207, s[44:47], s57 offen offset:1024 nt lds\n"
    "v_and_b32 v243, s74, v225\n"
    "v_mfma_f32_16x16x32_bf16 v[48:51], a[96:99], v[180:183], v[48:51]\n"
    "s_xor_b32 s57, s57, 57344\n"
    "v_exp_f32 v127, v127\n"
    "v_mfma_f32_16x16x32_bf16 v[52:55], a[96:99], v[184:187], v[52:55]\n"
    "s_add_u32 m0, s62, 8192\n"
    "ds_read_b64_tr_b16 a[88:89], v198 offset:32768\n"
    "v_cvt_pk_bf16_f32 v168, v120, v121\n"
    "v_mul_f32 v240, s65, v240\n"
    "v_mfma_f32_16x16x32_bf16 v[56:59], a[96:99], v[188:191], v[56:59]\n"
    "v_exp_f32 v128, v128\n"
    "v_mul_f32 v242, s65, v242\n"
    "v_mfma_f32_16x16x32_bf16 v[60:63], a[100:103], v[180:183], v[60:63]\n"
    "ds_read_b64_tr_b16 a[90:91], v198 offset:36864\n"
    "v_cvt_pk_bf16_f32 v169, v122, v123\n"
    "v_mfma_f32_16x16x32_bf16 v[64:67], a[100:103], v[184:187], v[64:67]\n"
    "v_exp_f32 v129, v129\n"
    "v_mul_f32 v241, s65, v241\n"
    "v_mfma_f32_16x16x32_bf16 v[68:71], a[100:103], v[188:191], v[68:71]\n"
    "ds_read_b64_tr_b16 a[92:93], v199 offset:32768\n"
    "v_cvt_pk_bf16_f32 v170, v124, v125\n"
    "v_mfma_f32_16x16x32_bf16 v[72:75], a[104:107], v[180:183], v[72:75]\n"
    "v_exp_f32 v130, v130\n"
    "v_mul_f32 v243, s65, v243\n"
    "v_mfma_f32_16x16x32_bf16 v[76:79], a[104:107], v[184:187], v[76:79]\n"
    "ds_read_b64_tr_b16 a[94:95], v199 offset:36864\n"
    "v_cvt_pk_bf16_f32 v171, v126, v127\n"
    "v_mfma_f32_16x16x32_bf16 v[80:83], a[104:107], v[188:191], v[80:83]\n"
    "v_exp_f32 v131, v131\n"
    "v_cvt_pk_bf16_f32 v224, v240, v241\n"
    "v_mfma_f32_16x16x32_bf16 v[84:87], a[108:111], v[180:183], v[84:87]\n"
    "buffer_load_dwordx4 v210, s[48:51], s60 offen offset:2048 nt lds\n"
    "v_cvt_pk_bf16_f32 v225, v242, v243\n"
    "v_mfma_f32_16x16x32_bf16 v[88:91], a[108:111], v[184:187], v[88:91]\n"
    "s_add_u32 m0, s61, 0\n"
    "v_exp_f32 v132, v132\n"
    "v_mfma_f32_16x16x32_bf16 v[92:95], a[108:111], v[188:191], v[92:95]\n"
    "ds_read_b64_tr_b16 a[96:97], v200 offset:32768\n"
    "v_cvt_pk_bf16_f32 v172, v128, v129\n"
    "v_lshlrev_b32 v244, 16, v226\n"
    "v_mfma_f32_16x16x32_bf16 v[144:147], a[48:51], a[0:3], v[108:111]\n"
    "v_exp_f32 v133, v133\n"
    "v_mfma_f32_16x16x32_bf16 v[144:147], a[52:55], a[4:7], v[144:147]\n"
    "ds_read_b64_tr_b16 a[98:99], v200 offset:36864\n"
    "v_cvt_pk_bf16_f32 v173, v130, v131\n"
    "v_lshlrev_b32 v246, 16, v227\n"
    "s_waitcnt lgkmcnt(3)\n"
    "v_mfma_f32_16x16x32_bf16 v[144:147], a[56:59], a[8:11], v[144:147]\n"
    "v_exp_f32 v134, v134\n"
    "v_mfma_f32_16x16x32_bf16 v[144:147], a[60:63], a[12:15], v[144:147]\n"
    "ds_read_b64_tr_b16 a[100:101], v201 offset:32768\n"
    "v_cvt_pk_bf16_f32 v174, v132, v133\n"
    "v_and_b32 v245, s74, v226\n"
    "v_mfma_f32_16x16x32_bf16 v[148:151], a[64:67], a[0:3], v[108:111]\n"
    "v_exp_f32 v135, v135\n"
    "v_and_b32 v247, s74, v227\n"
    "v_mfma_f32_16x16x32_bf16 v[148:151], a[68:71], a[4:7], v[148:151]\n"
    "ds_read_b64_tr_b16 a[102:103], v201 offset:36864\n"
    "v_cvt_pk_bf16_f32 v175, v134, v135\n"
    "v_mfma_f32_16x16x32_bf16 v[148:151], a[72:75], a[8:11], v[148:151]\n"
    "v_exp_f32 v136, v136\n"
    "v_mul_f32 v244, s65, v244\n"
    "v_mfma_f32_16x16x32_bf16 v[148:151], a[76:79], a[12:15], v[148:151]\n"
    "buffer_load_dwordx4 v204, s[40:43], s56 offen offset:0 nt lds\n"
    "v_mfma_f32_16x16x32_bf16 v[152:155], a[48:51], a[16:19], v[112:115]\n"
    "v_exp_f32 v137, v137\n"
    "v_mul_f32 v246, s65, v246\n"
    "v_mfma_f32_16x16x32_bf16 v[152:155], a[52:55], a[20:23], v[152:155]\n"
    "ds_read_b64_tr_b16 a[104:105], v202 offset:32768\n"
    "v_cvt_pk_bf16_f32 v176, v136, v137\n"
    "v_mfma_f32_16x16x32_bf16 v[152:155], a[56:59], a[24:27], v[152:155]\n"
    "v_exp_f32 v138, v138\n"
    "v_mul_f32 v245, s65, v245\n"
    "v_mfma_f32_16x16x32_bf16 v[152:155], a[60:63], a[28:31], v[152:155]\n"
    "ds_read_b64_tr_b16 a[106:107], v202 offset:36864\n"
    "ds_read_b64_tr_b16 a[108:109], v203 offset:32768\n"
    "v_mul_f32 v247, s65, v247\n"
    "v_mfma_f32_16x16x32_bf16 v[156:159], a[64:67], a[16:19], v[112:115]\n"
    "v_exp_f32 v139, v139\n"
    "v_mfma_f32_16x16x32_bf16 v[156:159], a[68:71], a[20:23], v[156:159]\n"
    "ds_read_b64_tr_b16 a[110:111], v203 offset:36864\n"
    "v_cvt_pk_bf16_f32 v177, v138, v139\n"
    "v_cvt_pk_bf16_f32 v226, v244, v245\n"
    "v_mfma_f32_16x16x32_bf16 v[156:159], a[72:75], a[24:27], v[156:159]\n"
    "v_exp_f32 v140, v140\n"
    "v_mfma_f32_16x16x32_bf16 v[156:159], a[76:79], a[28:31], v[156:159]\n"
    "v_cvt_pk_bf16_f32 v227, v246, v247\n"
    "v_mfma_f32_16x16x32_bf16 v[160:163], a[48:51], a[32:35], v[116:119]\n"
    "v_exp_f32 v141, v141\n"
    "v_mfma_f32_16x16x32_bf16 v[160:163], a[52:55], a[36:39], v[160:163]\n"
    "buffer_load_dwordx4 v205, s[40:43], s56 offen offset:1024 nt lds\n"
    "v_mfma_f32_16x16x32_bf16 v[160:163], a[56:59], a[40:43], v[160:163]\n"
    "s_xor_b32 s56, s56, 8192\n"
    "ds_write_b128 v212, v[224:227] offset:4096\n"
    "v_exp_f32 v142, v142\n"
    "v_mfma_f32_16x16x32_bf16 v[160:163], a[60:63], a[44:47], v[160:163]\n"
    "s_memrealtime s[94:95]\n"
    "v_cvt_pk_bf16_f32 v178, v140, v141\n"
    "v_mfma_f32_16x16x32_bf16 v[164:167], a[64:67], a[32:35], v[116:119]\n"
    "v_exp_f32 v143, v143\n"
    "v_mfma_f32_16x16x32_bf16 v[164:167], a[68:71], a[36:39], v[164:167]\n"
    "v_cvt_pk_bf16_f32 v179, v142, v143\n"
    "v_mfma_f32_16x16x32_bf16 v[164:167], a[72:75], a[40:43], v[164:167]\n"
    "v_mfma_f32_16x16x32_bf16 v[164:167], a[76:79], a[44:47], v[164:167]\n"
    "s_waitcnt vmcnt(10) lgkmcnt(0)\n"
    "v_mfma_f32_16x16x32_bf16 v[96:99], a[112:115], v[168:171], v[96:99]\n"
    "Lpc8_%=:\n"
    "s_sub_u32 s96, s94, s97\n"
    "s_cmp_lt_i32 s96, 0\n"
    "s_cbranch_scc1 Lsp8_%=\n"
    "s_barrier\n"
    "ds_read_b128 a[48:51], v192 offset:16384\n"
    "ds_read_b128 a[52:55], v193 offset:16384\n"
    "v_mfma_f32_16x16x32_bf16 v[100:103], a[112:115], v[172:175], v[100:103]\n"
    "s_add_u32 m0, s61, 32768\n"
    "ds_read_b128 v[228:231], v213 offset:4096\n"
    "v_exp_f32 v144, v144\n"
    "v_mfma_f32_16x16x32_bf16 v[104:107], a[112:115], v[176:179], v[104:107]\n"
    "s_sub_u32 s96, s96, 60\n"
    "ds_read_b128 a[56:59], v194 offset:16384\n"
    "ds_read_b128 a[60:63], v195 offset:16384\n"
    "v_mfma_f32_16x16x32_bf16 v[0:3], a[80:83], v[168:171], v[0:3]\n"
    "s_max_i32 s96, s96, 0\n"
    "v_exp_f32 v145, v145\n"
    "v_mfma_f32_16x16x32_bf16 v[4:7], a[80:83], v[172:175], v[4:7]\n"
    "s_add_u32 s97, s97, s96\n"
    "buffer_load_dwordx4 v206, s[44:47], s57 offen offset:0 nt lds\n"
    "v_mfma_f32_16x16x32_bf16 v[8:11], a[80:83], v[176:179], v[8:11]\n"
    "s_bfe_u32 s93, s78, 0x80008\n"
    "v_exp_f32 v146, v146\n"
    "v_mfma_f32_16x16x32_bf16 v[12:15], a[84:87], v[168:171], v[12:15]\n"
    "s_add_u32 s97, s97, s93\n"
    "ds_read_b128 a[64:67], v192 offset:20480\n"
    "ds_read_b128 a[68:71], v193 offset:20480\n"
    "v_mfma_f32_16x16x32_bf16 v[16:19], a[84:87], v[172:175], v[16:19]\n"
    "v_exp_f32 v147, v147\n"
    "v_mfma_f32_16x16x32_bf16 v[20:23], a[84:87], v[176:179], v[20:23]\n"
    "ds_read_b128 a[72:75], v194 offset:20480\n"
    "ds_read_b128 a[76:79], v195 offset:20480\n"
    "v_mfma_f32_16x16x32_bf16 v[24:27], a[88:91], v[168:171], v[24:27]\n"
    "v_exp_f32 v148, v148\n"
    "s_waitcnt lgkmcnt(6)\n"
    "v_lshlrev_b32 v240, 16, v228\n"
    "v_mfma_f32_16x16x32_bf16 v[28:31], a[88:91], v[172:175], v[28:31]\n"
    "ds_read_b64_tr_b16 a[80:81], v196 offset:40960\n"
    "ds_read_b64_tr_b16 a[82:83], v196 offset:45056\n"
    "v_lshlrev_b32 v242, 16, v229\n"
    "v_mfma_f32_16x16x32_bf16 v[32:35], a[88:91], v[176:179], v[32:35]\n"
    "v_exp_f32 v149, v149\n"
    "v_mfma_f32_16x16x32_bf16 v[36:39], a[92:95], v[168:171], v[36:39]\n"
    "ds_read_b64_tr_b16 a[84:85], v197 offset:40960\n"
    "ds_read_b64_tr_b16 a[86:87], v197 offset:45056\n"
    "v_and_b32 v241, s74, v228\n"
    "v_mfma_f32_16x16x32_bf16 v[40:43], a[92:95], v[172:175], v[40:43]\n"
    "v_exp_f32 v150, v150\n"
    "v_mfma_f32_16x16x32_bf16 v[44:47], a[92:95], v[176:179], v[44:47]\n"
    "buffer_load_dwordx4 v207, s[44:47], s57 offen offset:1024 nt lds\n"
    "v_and_b32 v243, s74, v229\n"
    "v_mfma_f32_16x16x32_bf16 v[48:51], a[96:99], v[168:171], v[48:51]\n"
    "s_xor_b32 s57, s57, 8192\n"
    "v_exp_f32 v151, v151\n"
    "v_mfma_f32_16x16x32_bf16 v[52:55], a[96:99], v[172:175], v[52:55]\n"
    "s_add_u32 m0, s62, 8192\n"
    "ds_read_b64_tr_b16 a[88:89], v198 offset:40960\n"
    "v_cvt_pk_bf16_f32 v180, v144, v145\n"
    "v_mul_f32 v240, s65, v240\n"
    "v_mfma_f32_16x16x32_bf16 v[56:59], a[96:99], v[176:179], v[56:59]\n"
    "v_exp_f32 v152, v152\n"
    "v_mul_f32 v242, s65, v242\n"
    "v_mfma_f32_16x16x32_bf16 v[60:63], a[100:103], v[168:171], v[60:63]\n"
    "ds_read_b64_tr_b16 a[90:91], v198 offset:45056\n"
    "v_cvt_pk_bf16_f32 v181, v146, v147\n"
    "v_mfma_f32_16x16x32_bf16 v[64:67], a[100:103], v[172:175], v[64:67]\n"
    "v_exp_f32 v153, v153\n"
    "v_mul_f32 v241, s65, v241\n"
    "v_mfma_f32_16x16x32_bf16 v[68:71], a[100:103], v[176:179], v[68:71]\n"
    "ds_read_b64_tr_b16 a[92:93], v199 offset:40960\n"
    "v_cvt_pk_bf16_f32 v182, v148, v149\n"
    "v_mfma_f32_16x16x32_bf16 v[72:75], a[104:107], v[168:171], v[72:75]\n"
    "v_exp_f32 v154, v154\n"
    "v_mul_f32 v243, s65, v243\n"
    "v_mfma_f32_16x16x32_bf16 v[76:79], a[104:107], v[172:175], v[76:79]\n"
    "ds_read_b64_tr_b16 a[94:95], v199 offset:45056\n"
    "v_cvt_pk_bf16_f32 v183, v150, v151\n"
    "v_mfma_f32_16x16x32_bf16 v[80:83], a[104:107], v[176:179], v[80:83]\n"
    "v_exp_f32 v155, v155\n"
    "v_cvt_pk_bf16_f32 v228, v240, v241\n"
    "v_mfma_f32_16x16x32_bf16 v[84:87], a[108:111], v[168:171], v[84:87]\n"
    "buffer_load_dwordx4 v211, s[48:51], s60 offen offset:3072 nt lds\n"
    "v_cvt_pk_bf16_f32 v229, v242, v243\n"
    "v_mfma_f32_16x16x32_bf16 v[88:91], a[108:111], v[172:175], v[88:91]\n"
    "s_add_u32 m0, s61, 8192\n"
    "v_exp_f32 v156, v156\n"
    "v_mfma_f32_16x16x32_bf16 v[92:95], a[108:111], v[176:179], v[92:95]\n"
    "ds_read_b64_tr_b16 a[96:97], v200 offset:40960\n"
    "v_cvt_pk_bf16_f32 v184, v152, v153\n"
    "v_lshlrev_b32 v244, 16, v230\n"
    "v_mfma_f32_16x16x32_bf16 v[120:123], a[48:51], a[0:3], v[108:111]\n"
    "v_exp_f32 v157, v157\n"
    "v_mfma_f32_16x16x32_bf16 v[120:123], a[52:55], a[4:7], v[120:123]\n"
    "ds_read_b64_tr_b16 a[98:99], v200 offset:45056\n"
    "v_cvt_pk_bf16_f32 v185, v154, v155\n"
    "v_lshlrev_b32 v246, 16, v231\n"
    "s_waitcnt lgkmcnt(3)\n"
    "v_mfma_f32_16x16x32_bf16 v[120:123], a[56:59], a[8:11], v[120:123]\n"
    "v_exp_f32 v158, v158\n"
    "v_mfma_f32_16x16x32_bf16 v[120:123], a[60:63], a[12:15], v[120:123]\n"
    "ds_read_b64_tr_b16 a[100:101], v201 offset:40960\n"
    "v_cvt_pk_bf16_f32 v186, v156, v157\n"
    "v_and_b32 v245, s74, v230\n"
    "v_mfma_f32_16x16x32_bf16 v[124:127], a[64:67], a[0:3], v[108:111]\n"
    "v_exp_f32 v159, v159\n"
    "v_and_b32 v247, s74, v231\n"
    "v_mfma_f32_16x16x32_bf16 v[124:127], a[68:71], a[4:7], v[124:127]\n"
    "ds_read_b64_tr_b16 a[102:103], v201 offset:45056\n"
    "v_cvt_pk_bf16_f32 v187, v158, v159\n"
    "v_mfma_f32_16x16x32_bf16 v[124:127], a[72:75], a[8:11], v[124:127]\n"
    "v_exp_f32 v160, v160\n"
    "v_mul_f32 v244, s65, v244\n"
    "v_mfma_f32_16x16x32_bf16 v[124:127], a[76:79], a[12:15], v[124:127]\n"
    "buffer_load_dwordx4 v204, s[40:43], s56 offen offset:0 nt lds\n"
    "v_mfma_f32_16x16x32_bf16 v[128:131], a[48:51], a[16:19], v[112:115]\n"
    "v_exp_f32 v161, v161\n"
    "v_mul_f32 v246, s65, v246\n"
    "v_mfma_f32_16x16x32_bf16 v[128:131], a[52:55], a[20:23], v[128:131]\n"
    "ds_read_b64_tr_b16 a[104:105], v202 offset:40960\n"
    "v_cvt_pk_bf16_f32 v188, v160, v161\n"
    "v_mfma_f32_16x16x32_bf16 v[128:131], a[56:59], a[24:27], v[128:131]\n"
    "v_exp_f32 v162, v162\n"
    "v_mul_f32 v245, s65, v245\n"
    "v_mfma_f32_16x16x32_bf16 v[128:131], a[60:63], a[28:31], v[128:131]\n"
    "ds_read_b64_tr_b16 a[106:107], v202 offset:45056\n"
    "ds_read_b64_tr_b16 a[108:109], v203 offset:40960\n"
    "v_mul_f32 v247, s65, v247\n"
    "v_mfma_f32_16x16x32_bf16 v[132:135], a[64:67], a[16:19], v[112:115]\n"
    "v_exp_f32 v163, v163\n"
    "v_mfma_f32_16x16x32_bf16 v[132:135], a[68:71], a[20:23], v[132:135]\n"
    "ds_read_b64_tr_b16 a[110:111], v203 offset:45056\n"
    "v_cvt_pk_bf16_f32 v189, v162, v163\n"
    "v_cvt_pk_bf16_f32 v230, v244, v245\n"
    "v_mfma_f32_16x16x32_bf16 v[132:135], a[72:75], a[24:27], v[132:135]\n"
    "v_exp_f32 v164, v164\n"
    "v_mfma_f32_16x16x32_bf16 v[132:135], a[76:79], a[28:31], v[132:135]\n"
    "v_cvt_pk_bf16_f32 v231, v246, v247\n"
    "v_mfma_f32_16x16x32_bf16 v[136:139], a[48:51], a[32:35], v[116:119]\n"
    "v_exp_f32 v165, v165\n"
    "v_mfma_f32_16x16x32_bf16 v[136:139], a[52:55], a[36:39], v[136:139]\n"
    "buffer_load_dwordx4 v205, s[40:43], s56 offen offset:1024 nt lds\n"
    "v_mfma_f32_16x16x32_bf16 v[136:139], a[56:59], a[40:43], v[136:139]\n"
    "s_xor_b32 s56, s56, 24576\n"
    "ds_write_b128 v213, v[228:231] offset:4096\n"
    "v_exp_f32 v166, v166\n"
    "v_mfma_f32_16x16x32_bf16 v[136:139], a[60:63], a[44:47], v[136:139]\n"
    "s_memrealtime s[94:95]\n"
    "v_cvt_pk_bf16_f32 v190, v164, v165\n"
    "v_mfma_f32_16x16x32_bf16 v[140:143], a[64:67], a[32:35], v[116:119]\n"
    "v_exp_f32 v167, v167\n"
    "v_mfma_f32_16x16x32_bf16 v[140:143], a[68:71], a[36:39], v[140:143]\n"
    "v_cvt_pk_bf16_f32 v191, v166, v167\n"
    "v_mfma_f32_16x16x32_bf16 v[140:143], a[72:75], a[40:43], v[140:143]\n"
    "v_mfma_f32_16x16x32_bf16 v[140:143], a[76:79], a[44:47], v[140:143]\n"
    "s_waitcnt vmcnt(10) lgkmcnt(0)\n"
    "v_mfma_f32_16x16x32_bf16 v[96:99], a[112:115], v[180:183], v[96:99]\n"
    "Lpc9_%=:\n"
    "s_sub_u32 s96, s94, s97\n"
    "s_cmp_lt_i32 s96, 0\n"
    "s_cbranch_scc1 Lsp9_%=\n"
    "s_barrier\n"
    "ds_read_b128 a[48:51], v192 offset:24576\n"
    "ds_read_b128 a[52:55], v193 offset:24576\n"
    "v_mfma_f32_16x16x32_bf16 v[100:103], a[112:115], v[184:187], v[100:103]\n"
    "s_add_u32 m0, s61, 40960\n"
    "ds_read_b128 v[232:235], v214 offset:4096\n"
    "v_exp_f32 v120, v120\n"
    "v_mfma_f32_16x16x32_bf16 v[104:107], a[112:115], v[188:191], v[104:107]\n"
    "s_sub_u32 s96, s96, 60\n"
    "ds_read_b128 a[56:59], v194 offset:24576\n"
    "ds_read_b128 a[60:63], v195 offset:24576\n"
    "v_mfma_f32_16x16x32_bf16 v[0:3], a[80:83], v[180:183], v[0:3]\n"
    "s_max_i32 s96, s96, 0\n"
    "v_exp_f32 v121, v121\n"
    "v_mfma_f32_16x16x32_bf16 v[4:7], a[80:83], v[184:187], v[4:7]\n"
    "s_add_u32 s97, s97, s96\n"
    "ds_read_b128 a[64:67], v192 offset:28672\n"
    "ds_read_b128 a[68:71], v193 offset:28672\n"
    "v_mfma_f32_16x16x32_bf16 v[8:11], a[80:83], v[188:191], v[8:11]\n"
    "s_bfe_u32 s93, s78, 0x80010\n"
    "v_exp_f32 v122, v122\n"
    "v_mfma_f32_16x16x32_bf16 v[12:15], a[84:87], v[180:183], v[12:15]\n"
    "s_add_u32 s97, s97, s93\n"
    "buffer_load_dwordx4 v206, s[44:47], s57 offen offset:0 nt lds\n"
    "v_mfma_f32_16x16x32_bf16 v[16:19], a[84:87], v[184:187], v[16:19]\n"
    "v_exp_f32 v123, v123\n"
    "v_mfma_f32_16x16x32_bf16 v[20:23], a[84:87], v[188:191], v[20:23]\n"
    "ds_read_b128 a[72:75], v194 offset:28672\n"
    "ds_read_b128 a[76:79], v195 offset:28672\n"
    "v_mfma_f32_16x16x32_bf16 v[24:27], a[88:91], v[180:183], v[24:27]\n"
    "v_exp_f32 v124, v124\n"
    "s_waitcnt lgkmcnt(6)\n"
    "v_lshlrev_b32 v240, 16, v232\n"
    "v_mfma_f32_16x16x32_bf16 v[28:31], a[88:91], v[184:187], v[28:31]\n"
    "ds_read_b64_tr_b16 a[80:81], v196 offset:49152\n"
    "ds_read_b64_tr_b16 a[82:83], v196 offset:53248\n"
    "v_lshlrev_b32 v242, 16, v233\n"
    "v_mfma_f32_16x16x32_bf16 v[32:35], a[88:91], v[188:191], v[32:35]\n"
    "v_exp_f32 v125, v125\n"
    "v_mfma_f32_16x16x32_bf16 v[36:39], a[92:95], v[180:183], v[36:39]\n"
    "ds_read_b64_tr_b16 a[84:85], v197 offset:49152\n"
    "v_cvt_pk_bf16_f32 v168, v120, v121\n"
    "v_and_b32 v241, s74, v232\n"
    "v_mfma_f32_16x16x32_bf16 v[40:43], a[92:95], v[184:187], v[40:43]\n"
    "v_exp_f32 v126, v126\n"
    "v_mfma_f32_16x16x32_bf16 v[44:47], a[92:95], v[188:191], v[44:47]\n"
    "ds_read_b64_tr_b16 a[86:87], v197 offset:53248\n"
    "v_cvt_pk_bf16_f32 v169, v122, v123\n"
    "v_and_b32 v243, s74, v233\n"
    "v_mfma_f32_16x16x32_bf16 v[48:51], a[96:99], v[180:183], v[48:51]\n"
    "v_exp_f32 v127, v127\n"
    "v_mfma_f32_16x16x32_bf16 v[52:55], a[96:99], v[184:187], v[52:55]\n"
    "ds_read_b64_tr_b16 a[88:89], v198 offset:49152\n"
    "v_cvt_pk_bf16_f32 v170, v124, v125\n"
    "v_mul_f32 v240, s65, v240\n"
    "v_mfma_f32_16x16x32_bf16 v[56:59], a[96:99], v[188:191], v[56:59]\n"
    "v_exp_f32 v128, v128\n"
    "v_mul_f32 v242, s65, v242\n"
    "v_mfma_f32_16x16x32_bf16 v[60:63], a[100:103], v[180:183], v[60:63]\n"
    "buffer_load_dwordx4 v207, s[44:47], s57 offen offset:1024 nt lds\n"
    "v_mfma_f32_16x16x32_bf16 v[64:67], a[100:103], v[184:187], v[64:67]\n"
    "s_xor_b32 s57, s57, 24576\n"
    "v_exp_f32 v129, v129\n"
    "v_mul_f32 v241, s65, v241\n"
    "v_mfma_f32_16x16x32_bf16 v[68:71], a[100:103], v[188:191], v[68:71]\n"
    "s_add_u32 m0, s61, 16384\n"
    "ds_read_b64_tr_b16 a[90:91], v198 offset:53248\n"
    "v_cvt_pk_bf16_f32 v171, v126, v127\n"
    "v_mfma_f32_16x16x32_bf16 v[72:75], a[104:107], v[180:183], v[72:75]\n"
    "v_exp_f32 v130, v130\n"
    "v_mul_f32 v243, s65, v243\n"
    "v_mfma_f32_16x16x32_bf16 v[76:79], a[104:107], v[184:187], v[76:79]\n"
    "ds_read_b64_tr_b16 a[92:93], v199 offset:49152\n"
    "v_cvt_pk_bf16_f32 v172, v128, v129\n"
    "v_mfma_f32_16x16x32_bf16 v[80:83], a[104:107], v[188:191], v[80:83]\n"
    "v_exp_f32 v131, v131\n"
    "v_cvt_pk_bf16_f32 v232, v240, v241\n"
    "v_mfma_f32_16x16x32_bf16 v[84:87], a[108:111], v[180:183], v[84:87]\n"
    "ds_read_b64_tr_b16 a[94:95], v199 offset:53248\n"
    "v_cvt_pk_bf16_f32 v173, v130, v131\n"
    "v_cvt_pk_bf16_f32 v233, v242, v243\n"
    "v_mfma_f32_16x16x32_bf16 v[88:91], a[108:111], v[184:187], v[88:91]\n"
    "v_exp_f32 v132, v132\n"
    "v_mfma_f32_16x16x32_bf16 v[92:95], a[108:111], v[188:191], v[92:95]\n"
    "ds_read_b64_tr_b16 a[96:97], v200 offset:49152\n"
    "ds_read_b64_tr_b16 a[98:99], v200 offset:53248\n"
    "v_lshlrev_b32 v244, 16, v234\n"
    "v_mfma_f32_16x16x32_bf16 v[144:147], a[48:51], a[0:3], v[108:111]\n"
    "v_exp_f32 v133, v133\n"
    "v_mfma_f32_16x16x32_bf16 v[144:147], a[52:55], a[4:7], v[144:147]\n"
    "ds_read_b64_tr_b16 a[100:101], v201 offset:49152\n"
    "v_cvt_pk_bf16_f32 v174, v132, v133\n"
    "v_lshlrev_b32 v246, 16, v235\n"
    "s_waitcnt lgkmcnt(5)\n"
    "v_mfma_f32_16x16x32_bf16 v[144:147], a[56:59], a[8:11], v[144:147]\n"
    "v_exp_f32 v134, v134\n"
    "v_mfma_f32_16x16x32_bf16 v[144:147], a[60:63], a[12:15], v[144:147]\n"
    "ds_read_b64_tr_b16 a[102:103], v201 offset:53248\n"
    "ds_read_b64_tr_b16 a[104:105], v202 offset:49152\n"
    "v_and_b32 v245, s74, v234\n"
    "v_mfma_f32_16x16x32_bf16 v[148:151], a[64:67], a[0:3], v[108:111]\n"
    "v_exp_f32 v135, v135\n"
    "v_and_b32 v247, s74, v235\n"
    "v_mfma_f32_16x16x32_bf16 v[148:151], a[68:71], a[4:7], v[148:151]\n"
    "buffer_load_dwordx4 v204, s[40:43], s56 offen offset:0 nt lds\n"
    "v_mfma_f32_16x16x32_bf16 v[148:151], a[72:75], a[8:11], v[148:151]\n"
    "v_exp_f32 v136, v136\n"
    "v_mul_f32 v244, s65, v244\n"
    "v_mfma_f32_16x16x32_bf16 v[148:151], a[76:79], a[12:15], v[148:151]\n"
    "ds_read_b64_tr_b16 a[106:107], v202 offset:53248\n"
    "v_cvt_pk_bf16_f32 v175, v134, v135\n"
    "v_mfma_f32_16x16x32_bf16 v[152:155], a[48:51], a[16:19], v[112:115]\n"
    "v_exp_f32 v137, v137\n"
    "v_mul_f32 v246, s65, v246\n"
    "v_mfma_f32_16x16x32_bf16 v[152:155], a[52:55], a[20:23], v[152:155]\n"
    "ds_read_b64_tr_b16 a[108:109], v203 offset:49152\n"
    "v_cvt_pk_bf16_f32 v176, v136, v137\n"
    "v_mfma_f32_16x16x32_bf16 v[152:155], a[56:59], a[24:27], v[152:155]\n"
    "v_exp_f32 v138, v138\n"
    "v_mul_f32 v245, s65, v245\n"
    "v_mfma_f32_16x16x32_bf16 v[152:155], a[60:63], a[28:31], v[152:155]\n"
    "ds_read_b64_tr_b16 a[110:111], v203 offset:53248\n"
    "v_mul_f32 v247, s65, v247\n"
    "v_mfma_f32_16x16x32_bf16 v[156:159], a[64:67], a[16:19], v[112:115]\n"
    "v_exp_f32 v139, v139\n"
    "v_mfma_f32_16x16x32_bf16 v[156:159], a[68:71], a[20:23], v[156:159]\n"
    "v_cvt_pk_bf16_f32 v177, v138, v139\n"
    "v_cvt_pk_bf16_f32 v234, v244, v245\n"
    "v_mfma_f32_16x16x32_bf16 v[156:159], a[72:75], a[24:27], v[156:159]\n"
    "v_exp_f32 v140, v140\n"
    "v_mfma_f32_16x16x32_bf16 v[156:159], a[76:79], a[28:31], v[156:159]\n"
    "v_cvt_pk_bf16_f32 v235, v246, v247\n"
    "v_mfma_f32_16x16x32_bf16 v[160:163], a[48:51], a[32:35], v[116:119]\n"
    "v_exp_f32 v141, v141\n"
    "v_mfma_f32_16x16x32_bf16 v[160:163], a[52:55], a[36:39], v[160:163]\n"
    "buffer_load_dwordx4 v205, s[40:43], s56 offen offset:1024 nt lds\n"
    "v_mfma_f32_16x16x32_bf16 v[160:163], a[56:59], a[40:43], v[160:163]\n"
    "s_xor_b32 s56, s56, 8192\n"
    "ds_write_b128 v214, v[232:235] offset:4096\n"
    "v_exp_f32 v142, v142\n"
    "v_mfma_f32_16x16x32_bf16 v[160:163], a[60:63], a[44:47], v[160:163]\n"
    "s_memrealtime s[94:95]\n"
    "v_cvt_pk_bf16_f32 v178, v140, v141\n"
    "v_mfma_f32_16x16x32_bf16 v[164:167], a[64:67], a[32:35], v[116:119]\n"
    "v_exp_f32 v143, v143\n"
    "v_mfma_f32_16x16x32_bf16 v[164:167], a[68:71], a[36:39], v[164:167]\n"
    "v_cvt_pk_bf16_f32 v179, v142, v143\n"
    "v_mfma_f32_16x16x32_bf16 v[164:167], a[72:75], a[40:43], v[164:167]\n"
    "v_mfma_f32_16x16x32_bf16 v[164:167], a[76:79], a[44:47], v[164:167]\n"
    "s_waitcnt vmcnt(9) lgkmcnt(0)\n"
    "v_mfma_f32_16x16x32_bf16 v[96:99], a[112:115], v[168:171], v[96:99]\n"
    "Lpc10_%=:\n"
    "s_sub_u32 s96, s94, s97\n"
    "s_cmp_lt_i32 s96, 0\n"
    "s_cbranch_scc1 Lsp10_%=\n"
    "s_barrier\n"
    "ds_read_b128 a[48:51], v192 offset:0\n"
    "ds_read_b128 a[52:55], v193 offset:0\n"
    "v_mfma_f32_16x16x32_bf16 v[100:103], a[112:115], v[172:175], v[100:103]\n"
    "s_add_u32 m0, s61, 49152\n"
    "ds_read_b128 v[236:239], v215 offset:4096\n"
    "v_exp_f32 v144, v144\n"
    "v_mfma_f32_16x16x32_bf16 v[104:107], a[112:115], v[176:179], v[104:107]\n"
    "s_sub_u32 s96, s96, 60\n"
    "ds_read_b128 a[56:59], v194 offset:0\n"
    "ds_read_b128 a[60:63], v195 offset:0\n"
    "v_mfma_f32_16x16x32_bf16 v[0:3], a[80:83], v[168:171], v[0:3]\n"
    "s_max_i32 s96, s96, 0\n"
    "v_exp_f32 v145, v145\n"
    "v_mfma_f32_16x16x32_bf16 v[4:7], a[80:83], v[172:175], v[4:7]\n"
    "s_add_u32 s97, s97, s96\n"
    "ds_read_b128 a[64:67], v192 offset:4096\n"
    "ds_read_b128 a[68:71], v193 offset:4096\n"
    "v_mfma_f32_16x16x32_bf16 v[8:11], a[80:83], v[176:179], v[8:11]\n"
    "s_bfe_u32 s93, s78, 0x80018\n"
    "v_exp_f32 v146, v146\n"
    "v_mfma_f32_16x16x32_bf16 v[12:15], a[84:87], v[168:171], v[12:15]\n"
    "s_add_u32 s97, s97, s93\n"
    "buffer_load_dwordx4 v206, s[44:47], s57 offen offset:0 nt lds\n"
    "v_mfma_f32_16x16x32_bf16 v[16:19], a[84:87], v[172:175], v[16:19]\n"
    "v_exp_f32 v147, v147\n"
    "v_mfma_f32_16x16x32_bf16 v[20:23], a[84:87], v[176:179], v[20:23]\n"
    "ds_read_b128 a[72:75], v194 offset:4096\n"
    "ds_read_b128 a[76:79], v195 offset:4096\n"
    "v_mfma_f32_16x16x32_bf16 v[24:27], a[88:91], v[168:171], v[24:27]\n"
    "v_exp_f32 v148, v148\n"
    "s_waitcnt lgkmcnt(6)\n"
    "v_lshlrev_b32 v240, 16, v236\n"
    "v_mfma_f32_16x16x32_bf16 v[28:31], a[88:91], v[172:175], v[28:31]\n"
    "ds_read_b64_tr_b16 a[80:81], v196 offset:57344\n"
    "ds_read_b64_tr_b16 a[82:83], v196 offset:61440\n"
    "v_lshlrev_b32 v242, 16, v237\n"
    "v_mfma_f32_16x16x32_bf16 v[32:35], a[88:91], v[176:179], v[32:35]\n"
    "v_exp_f32 v149, v149\n"
    "v_mfma_f32_16x16x32_bf16 v[36:39], a[92:95], v[168:171], v[36:39]\n"
    "ds_read_b64_tr_b16 a[84:85], v197 offset:57344\n"
    "v_cvt_pk_bf16_f32 v180, v144, v145\n"
    "v_and_b32 v241, s74, v236\n"
    "v_mfma_f32_16x16x32_bf16 v[40:43], a[92:95], v[172:175], v[40:43]\n"
    "v_exp_f32 v150, v150\n"
    "v_mfma_f32_16x16x32_bf16 v[44:47], a[92:95], v[176:179], v[44:47]\n"
    "ds_read_b64_tr_b16 a[86:87], v197 offset:61440\n"
    "v_cvt_pk_bf16_f32 v181, v146, v147\n"
    "v_and_b32 v243, s74, v237\n"
    "v_mfma_f32_16x16x32_bf16 v[48:51], a[96:99], v[168:171], v[48:51]\n"
    "v_exp_f32 v151, v151\n"
    "v_mfma_f32_16x16x32_bf16 v[52:55], a[96:99], v[172:175], v[52:55]\n"
    "ds_read_b64_tr_b16 a[88:89], v198 offset:57344\n"
    "v_cvt_pk_bf16_f32 v182, v148, v149\n"
    "v_mul_f32 v240, s65, v240\n"
    "v_mfma_f32_16x16x32_bf16 v[56:59], a[96:99], v[176:179], v[56:59]\n"
    "v_exp_f32 v152, v152\n"
    "v_mul_f32 v242, s65, v242\n"
    "v_mfma_f32_16x16x32_bf16 v[60:63], a[100:103], v[168:171], v[60:63]\n"
    "buffer_load_dwordx4 v207, s[44:47], s57 offen offset:1024 nt lds\n"
    "v_mfma_f32_16x16x32_bf16 v[64:67], a[100:103], v[172:175], v[64:67]\n"
    "s_xor_b32 s57, s57, 8192\n"
    "v_exp_f32 v153, v153\n"
    "v_mul_f32 v241, s65, v241\n"
    "v_mfma_f32_16x16x32_bf16 v[68:71], a[100:103], v[176:179], v[68:71]\n"
    "s_add_u32 m0, s61, 24576\n"
    "ds_read_b64_tr_b16 a[90:91], v198 offset:61440\n"
    "v_cvt_pk_bf16_f32 v183, v150, v151\n"
    "v_mfma_f32_16x16x32_bf16 v[72:75], a[104:107], v[168:171], v[72:75]\n"
    "v_exp_f32 v154, v154\n"
    "v_mul_f32 v243, s65, v243\n"
    "v_mfma_f32_16x16x32_bf16 v[76:79], a[104:107], v[172:175], v[76:79]\n"
    "ds_read_b64_tr_b16 a[92:93], v199 offset:57344\n"
    "v_cvt_pk_bf16_f32 v184, v152, v153\n"
    "v_mfma_f32_16x16x32_bf16 v[80:83], a[104:107], v[176:179], v[80:83]\n"
    "v_exp_f32 v155, v155\n"
    "v_cvt_pk_bf16_f32 v236, v240, v241\n"
    "v_mfma_f32_16x16x32_bf16 v[84:87], a[108:111], v[168:171], v[84:87]\n"
    "ds_read_b64_tr_b16 a[94:95], v199 offset:61440\n"
    "v_cvt_pk_bf16_f32 v185, v154, v155\n"
    "v_cvt_pk_bf16_f32 v237, v242, v243\n"
    "v_mfma_f32_16x16x32_bf16 v[88:91], a[108:111], v[172:175], v[88:91]\n"
    "v_exp_f32 v156, v156\n"
    "v_mfma_f32_16x16x32_bf16 v[92:95], a[108:111], v[176:179], v[92:95]\n"
    "ds_read_b64_tr_b16 a[96:97], v200 offset:57344\n"
    "ds_read_b64_tr_b16 a[98:99], v200 offset:61440\n"
    "v_lshlrev_b32 v244, 16, v238\n"
    "v_mfma_f32_16x16x32_bf16 v[120:123], a[48:51], a[0:3], v[108:111]\n"
    "v_exp_f32 v157, v157\n"
    "v_mfma_f32_16x16x32_bf16 v[120:123], a[52:55], a[4:7], v[120:123]\n"
    "ds_read_b64_tr_b16 a[100:101], v201 offset:57344\n"
    "v_cvt_pk_bf16_f32 v186, v156, v157\n"
    "v_lshlrev_b32 v246, 16, v239\n"
    "s_waitcnt lgkmcnt(5)\n"
    "v_mfma_f32_16x16x32_bf16 v[120:123], a[56:59], a[8:11], v[120:123]\n"
    "v_exp_f32 v158, v158\n"
    "v_mfma_f32_16x16x32_bf16 v[120:123], a[60:63], a[12:15], v[120:123]\n"
    "ds_read_b64_tr_b16 a[102:103], v201 offset:61440\n"
    "ds_read_b64_tr_b16 a[104:105], v202 offset:57344\n"
    "v_and_b32 v245, s74, v238\n"
    "v_mfma_f32_16x16x32_bf16 v[124:127], a[64:67], a[0:3], v[108:111]\n"
    "v_exp_f32 v159, v159\n"
    "v_and_b32 v247, s74, v239\n"
    "v_mfma_f32_16x16x32_bf16 v[124:127], a[68:71], a[4:7], v[124:127]\n"
    "buffer_load_dwordx4 v204, s[40:43], s56 offen offset:0 nt lds\n"
    "v_mfma_f32_16x16x32_bf16 v[124:127], a[72:75], a[8:11], v[124:127]\n"
    "v_exp_f32 v160, v160\n"
    "v_mul_f32 v244, s65, v244\n"
    "v_mfma_f32_16x16x32_bf16 v[124:127], a[76:79], a[12:15], v[124:127]\n"
    "ds_read_b64_tr_b16 a[106:107], v202 offset:61440\n"
    "v_cvt_pk_bf16_f32 v187, v158, v159\n"
    "v_mfma_f32_16x16x32_bf16 v[128:131], a[48:51], a[16:19], v[112:115]\n"
    "v_exp_f32 v161, v161\n"
    "v_mul_f32 v246, s65, v246\n"
    "v_mfma_f32_16x16x32_bf16 v[128:131], a[52:55], a[20:23], v[128:131]\n"
    "ds_read_b64_tr_b16 a[108:109], v203 offset:57344\n"
    "v_cvt_pk_bf16_f32 v188, v160, v161\n"
    "v_mfma_f32_16x16x32_bf16 v[128:131], a[56:59], a[24:27], v[128:131]\n"
    "v_exp_f32 v162, v162\n"
    "v_mul_f32 v245, s65, v245\n"
    "v_mfma_f32_16x16x32_bf16 v[128:131], a[60:63], a[28:31], v[128:131]\n"
    "ds_read_b64_tr_b16 a[110:111], v203 offset:61440\n"
    "v_mul_f32 v247, s65, v247\n"
    "v_mfma_f32_16x16x32_bf16 v[132:135], a[64:67], a[16:19], v[112:115]\n"
    "v_exp_f32 v163, v163\n"
    "v_mfma_f32_16x16x32_bf16 v[132:135], a[68:71], a[20:23], v[132:135]\n"
    "v_cvt_pk_bf16_f32 v189, v162, v163\n"
    "v_cvt_pk_bf16_f32 v238, v244, v245\n"
    "v_mfma_f32_16x16x32_bf16 v[132:135], a[72:75], a[24:27], v[132:135]\n"
    "v_exp_f32 v164, v164\n"
    "v_mfma_f32_16x16x32_bf16 v[132:135], a[76:79], a[28:31], v[132:135]\n"
    "v_cvt_pk_bf16_f32 v239, v246, v247\n"
    "v_mfma_f32_16x16x32_bf16 v[136:139], a[48:51], a[32:35], v[116:119]\n"
    "v_exp_f32 v165, v165\n"
    "v_mfma_f32_16x16x32_bf16 v[136:139], a[52:55], a[36:39], v[136:139]\n"
    "buffer_load_dwordx4 v205, s[40:43], s56 offen offset:1024 nt lds\n"
    "v_mfma_f32_16x16x32_bf16 v[136:139], a[56:59], a[40:43], v[136:139]\n"
    "s_xor_b32 s56, s56, 122880\n"
    "ds_write_b128 v215, v[236:239] offset:4096\n"
    "v_exp_f32 v166, v166\n"
    "v_mfma_f32_16x16x32_bf16 v[136:139], a[60:63], a[44:47], v[136:139]\n"
    "s_memrealtime s[94:95]\n"
    "v_cvt_pk_bf16_f32 v190, v164, v165\n"
    "v_mfma_f32_16x16x32_bf16 v[140:143], a[64:67], a[32:35], v[116:119]\n"
    "v_exp_f32 v167, v167\n"
    "v_mfma_f32_16x16x32_bf16 v[140:143], a[68:71], a[36:39], v[140:143]\n"
    "v_cvt_pk_bf16_f32 v191, v166, v167\n"
    "v_mfma_f32_16x16x32_bf16 v[140:143], a[72:75], a[40:43], v[140:143]\n"
    "v_mfma_f32_16x16x32_bf16 v[140:143], a[76:79], a[44:47], v[140:143]\n"
    "s_waitcnt vmcnt(8) lgkmcnt(0)\n"
    "s_waitcnt vmcnt(10)\n"
    "v_mfma_f32_16x16x32_bf16 v[96:99], a[112:115], v[180:183], v[96:99]\n"
    "Lpc11_%=:\n"
    "s_sub_u32 s96, s94, s97\n"
    "s_cmp_lt_i32 s96, 0\n"
    "s_cbranch_scc1 Lsp11_%=\n"
    "s_barrier\n"
    "ds_read_b128 a[48:51], v192 offset:8192\n"
    "ds_read_b128 a[52:55], v193 offset:8192\n"
    "v_mfma_f32_16x16x32_bf16 v[100:103], a[112:115], v[184:187], v[100:103]\n"
    "s_add_u32 m0, s61, 57344\n"
    "ds_read_b128 v[224:227], v212 offset:8192\n"
    "v_exp_f32 v120, v120\n"
    "v_mfma_f32_16x16x32_bf16 v[104:107], a[112:115], v[188:191], v[104:107]\n"
    "s_sub_u32 s96, s96, 60\n"
    "ds_read_b128 a[56:59], v194 offset:8192\n"
    "ds_read_b128 a[60:63], v195 offset:8192\n"
    "v_mfma_f32_16x16x32_bf16 v[0:3], a[80:83], v[180:183], v[0:3]\n"
    "s_max_i32 s96, s96, 0\n"
    "v_exp_f32 v121, v121\n"
    "v_mfma_f32_16x16x32_bf16 v[4:7], a[80:83], v[184:187], v[4:7]\n"
    "s_add_u32 s97, s97, s96\n"
    "ds_read_b128 a[64:67], v192 offset:12288\n"
    "ds_read_b128 a[68:71], v193 offset:12288\n"
    "v_mfma_f32_16x16x32_bf16 v[8:11], a[80:83], v[188:191], v[8:11]\n"
    "s_bfe_u32 s93, s79, 0x80000\n"
    "v_exp_f32 v122, v122\n"
    "v_mfma_f32_16x16x32_bf16 v[12:15], a[84:87], v[180:183], v[12:15]\n"
    "s_add_u32 s97, s97, s93\n"
    "buffer_load_dwordx4 v206, s[44:47], s57 offen offset:0 nt lds\n"
    "v_mfma_f32_16x16x32_bf16 v[16:19], a[84:87], v[184:187], v[16:19]\n"
    "v_exp_f32 v123, v123\n"
    "v_mfma_f32_16x16x32_bf16 v[20:23], a[84:87], v[188:191], v[20:23]\n"
    "ds_read_b128 a[72:75], v194 offset:12288\n"
    "ds_read_b128 a[76:79], v195 offset:12288\n"
    "v_mfma_f32_16x16x32_bf16 v[24:27], a[88:91], v[180:183], v[24:27]\n"
    "v_exp_f32 v124, v124\n"
    "s_waitcnt lgkmcnt(6)\n"
    "v_lshlrev_b32 v240, 16, v224\n"
    "v_lshlrev_b32 v242, 16, v225\n"
    "v_mfma_f32_16x16x32_bf16 v[28:31], a[88:91], v[184:187], v[28:31]\n"
    "ds_read_b64_tr_b16 a[80:81], v196 offset:32768\n"
    "ds_read_b64_tr_b16 a[82:83], v196 offset:36864\n"
    "v_and_b32 v241, s74, v224\n"
    "v_and_b32 v243, s74, v225\n"
    "v_mfma_f32_16x16x32_bf16 v[32:35], a[88:91], v[188:191], v[32:35]\n"
    "v_exp_f32 v125, v125\n"
    "v_mul_f32 v240, s65, v240\n"
    "v_mfma_f32_16x16x32_bf16 v[36:39], a[92:95], v[180:183], v[36:39]\n"
    "ds_read_b64_tr_b16 a[84:85], v197 offset:32768\n"
    "v_cvt_pk_bf16_f32 v168, v120, v121\n"
    "v_mul_f32 v242, s65, v242\n"
    "v_mul_f32 v241, s65, v241\n"
    "v_mfma_f32_16x16x32_bf16 v[40:43], a[92:95], v[184:187], v[40:43]\n"
    "v_exp_f32 v126, v126\n"
    "v_mul_f32 v243, s65, v243\n"
    "v_cvt_pk_bf16_f32 v224, v240, v241\n"
    "v_mfma_f32_16x16x32_bf16 v[44:47], a[92:95], v[188:191], v[44:47]\n"
    "ds_read_b64_tr_b16 a[86:87], v197 offset:36864\n"
    "v_cvt_pk_bf16_f32 v169, v122, v123\n"
    "v_cvt_pk_bf16_f32 v225, v242, v243\n"
    "v_mfma_f32_16x16x32_bf16 v[48:51], a[96:99], v[180:183], v[48:51]\n"
    "v_exp_f32 v127, v127\n"
    "v_lshlrev_b32 v244, 16, v226\n"
    "v_lshlrev_b32 v246, 16, v227\n"
    "v_mfma_f32_16x16x32_bf16 v[52:55], a[96:99], v[184:187], v[52:55]\n"
    "ds_read_b64_tr_b16 a[88:89], v198 offset:32768\n"
    "v_cvt_pk_bf16_f32 v170, v124, v125\n"
    "v_and_b32 v245, s74, v226\n"
    "v_and_b32 v247, s74, v227\n"
    "v_mfma_f32_16x16x32_bf16 v[56:59], a[96:99], v[188:191], v[56:59]\n"
    "v_exp_f32 v128, v128\n"
    "v_mul_f32 v244, s65, v244\n"
    "v_mfma_f32_16x16x32_bf16 v[60:63], a[100:103], v[180:183], v[60:63]\n"
    "buffer_load_dwordx4 v207, s[44:47], s57 offen offset:1024 nt lds\n"
    "v_mul_f32 v246, s65, v246\n"
    "v_mul_f32 v245, s65, v245\n"
    "v_mfma_f32_16x16x32_bf16 v[64:67], a[100:103], v[184:187], v[64:67]\n"
    "s_xor_b32 s57, s57, 122880\n"
    "v_exp_f32 v129, v129\n"
    "v_mul_f32 v247, s65, v247\n"
    "v_cvt_pk_bf16_f32 v226, v244, v245\n"
    "v_mfma_f32_16x16x32_bf16 v[68:71], a[100:103], v[188:191], v[68:71]\n"
    "s_add_u32 s40, s40, 33554432\n"
    "s_addc_u32 s41, s41, 0\n"
    "ds_read_b64_tr_b16 a[90:91], v198 offset:36864\n"
    "v_cvt_pk_bf16_f32 v171, v126, v127\n"
    "v_cvt_pk_bf16_f32 v227, v246, v247\n"
    "v_mfma_f32_16x16x32_bf16 v[72:75], a[104:107], v[180:183], v[72:75]\n"
    "s_cmp_lt_u32 s63, 3\n"
    "s_cselect_b32 s42, 131072, 0\n"
    "v_exp_f32 v130, v130\n"
    "v_mfma_f32_16x16x32_bf16 v[76:79], a[104:107], v[184:187], v[76:79]\n"
    "s_add_u32 m0, s61, 0\n"
    "ds_read_b64_tr_b16 a[92:93], v199 offset:32768\n"
    "ds_write_b128 v212, v[224:227] offset:8192\n"
    "v_cvt_pk_bf16_f32 v172, v128, v129\n"
    "v_mfma_f32_16x16x32_bf16 v[80:83], a[104:107], v[188:191], v[80:83]\n"
    "v_exp_f32 v131, v131\n"
    "v_mfma_f32_16x16x32_bf16 v[84:87], a[108:111], v[180:183], v[84:87]\n"
    "ds_read_b64_tr_b16 a[94:95], v199 offset:36864\n"
    "v_cvt_pk_bf16_f32 v173, v130, v131\n"
    "v_mfma_f32_16x16x32_bf16 v[88:91], a[108:111], v[184:187], v[88:91]\n"
    "ds_read_b128 v[228:231], v213 offset:8192\n"
    "v_exp_f32 v132, v132\n"
    "v_mfma_f32_16x16x32_bf16 v[92:95], a[108:111], v[188:191], v[92:95]\n"
    "ds_read_b64_tr_b16 a[96:97], v200 offset:32768\n"
    "ds_read_b64_tr_b16 a[98:99], v200 offset:36864\n"
    "v_mfma_f32_16x16x32_bf16 v[144:147], a[48:51], a[0:3], v[108:111]\n"
    "v_exp_f32 v133, v133\n"
    "v_mfma_f32_16x16x32_bf16 v[144:147], a[52:55], a[4:7], v[144:147]\n"
    "ds_read_b64_tr_b16 a[100:101], v201 offset:32768\n"
    "v_cvt_pk_bf16_f32 v174, v132, v133\n"
    "s_waitcnt lgkmcnt(7)\n"
    "v_mfma_f32_16x16x32_bf16 v[144:147], a[56:59], a[8:11], v[144:147]\n"
    "v_exp_f32 v134, v134\n"
    "v_mfma_f32_16x16x32_bf16 v[144:147], a[60:63], a[12:15], v[144:147]\n"
    "ds_read_b64_tr_b16 a[102:103], v201 offset:36864\n"
    "ds_read_b64_tr_b16 a[104:105], v202 offset:32768\n"
    "v_mfma_f32_16x16x32_bf16 v[148:151], a[64:67], a[0:3], v[108:111]\n"
    "v_exp_f32 v135, v135\n"
    "v_mfma_f32_16x16x32_bf16 v[148:151], a[68:71], a[4:7], v[148:151]\n"
    "buffer_load_dwordx4 v204, s[40:43], s56 offen offset:0 nt lds\n"
    "v_mfma_f32_16x16x32_bf16 v[148:151], a[72:75], a[8:11], v[148:151]\n"
    "v_exp_f32 v136, v136\n"
    "s_waitcnt lgkmcnt(5)\n"
    "v_lshlrev_b32 v240, 16, v228\n"
    "v_lshlrev_b32 v242, 16, v229\n"
    "v_mfma_f32_16x16x32_bf16 v[148:151], a[76:79], a[12:15], v[148:151]\n"
    "ds_read_b64_tr_b16 a[106:107], v202 offset:36864\n"
    "v_cvt_pk_bf16_f32 v175, v134, v135\n"
    "v_and_b32 v241, s74, v228\n"
    "v_and_b32 v243, s74, v229\n"
    "v_mfma_f32_16x16x32_bf16 v[152:155], a[48:51], a[16:19], v[112:115]\n"
    "v_exp_f32 v137, v137\n"
    "v_mul_f32 v240, s65, v240\n"
    "v_mul_f32 v242, s65, v242\n"
    "v_mfma_f32_16x16x32_bf16 v[152:155], a[52:55], a[20:23], v[152:155]\n"
    "ds_read_b64_tr_b16 a[108:109], v203 offset:32768\n"
    "v_cvt_pk_bf16_f32 v176, v136, v137\n"
    "v_mul_f32 v241, s65, v241\n"
    "v_mul_f32 v243, s65, v243\n"
    "v_mfma_f32_16x16x32_bf16 v[152:155], a[56:59], a[24:27], v[152:155]\n"
    "v_exp_f32 v138, v138\n"
    "v_cvt_pk_bf16_f32 v228, v240, v241\n"
    "v_cvt_pk_bf16_f32 v229, v242, v243\n"
    "v_mfma_f32_16x16x32_bf16 v[152:155], a[60:63], a[28:31], v[152:155]\n"
    "ds_read_b64_tr_b16 a[110:111], v203 offset:36864\n"
    "v_lshlrev_b32 v244, 16, v230\n"
    "v_mfma_f32_16x16x32_bf16 v[156:159], a[64:67], a[16:19], v[112:115]\n"
    "v_exp_f32 v139, v139\n"
    "v_lshlrev_b32 v246, 16, v231\n"
    "v_and_b32 v245, s74, v230\n"
    "v_mfma_f32_16x16x32_bf16 v[156:159], a[68:71], a[20:23], v[156:159]\n"
    "v_cvt_pk_bf16_f32 v177, v138, v139\n"
    "v_and_b32 v247, s74, v231\n"
    "v_mul_f32 v244, s65, v244\n"
    "v_mfma_f32_16x16x32_bf16 v[156:159], a[72:75], a[24:27], v[156:159]\n"
    "v_exp_f32 v140, v140\n"
    "v_mul_f32 v246, s65, v246\n"
    "v_mul_f32 v245, s65, v245\n"
    "v_mfma_f32_16x16x32_bf16 v[156:159], a[76:79], a[28:31], v[156:159]\n"
    "v_mul_f32 v247, s65, v247\n"
    "v_cvt_pk_bf16_f32 v230, v244, v245\n"
    "v_mfma_f32_16x16x32_bf16 v[160:163], a[48:51], a[32:35], v[116:119]\n"
    "v_exp_f32 v141, v141\n"
    "v_cvt_pk_bf16_f32 v231, v246, v247\n"
    "v_mfma_f32_16x16x32_bf16 v[160:163], a[52:55], a[36:39], v[160:163]\n"
    "buffer_load_dwordx4 v205, s[40:43], s56 offen offset:1024 nt lds\n"
    "v_mfma_f32_16x16x32_bf16 v[160:163], a[56:59], a[40:43], v[160:163]\n"
    "s_xor_b32 s56, s56, 8192\n"
    "ds_write_b128 v213, v[228:231] offset:8192\n"
    "v_exp_f32 v142, v142\n"
    "v_mfma_f32_16x16x32_bf16 v[160:163], a[60:63], a[44:47], v[160:163]\n"
    "s_memrealtime s[94:95]\n"
    "v_cvt_pk_bf16_f32 v178, v140, v141\n"
    "v_mfma_f32_16x16x32_bf16 v[164:167], a[64:67], a[32:35], v[116:119]\n"
    "v_exp_f32 v143, v143\n"
    "v_mfma_f32_16x16x32_bf16 v[164:167], a[68:71], a[36:39], v[164:167]\n"
    "v_cvt_pk_bf16_f32 v179, v142, v143\n"
    "v_mfma_f32_16x16x32_bf16 v[164:167], a[72:75], a[40:43], v[164:167]\n"
    "v_mfma_f32_16x16x32_bf16 v[164:167], a[76:79], a[44:47], v[164:167]\n"
    "s_waitcnt vmcnt(8) lgkmcnt(0)\n"
    "v_mfma_f32_16x16x32_bf16 v[96:99], a[112:115], v[168:171], v[96:99]\n"
    "Lpc12_%=:\n"
    "s_sub_u32 s96, s94, s97\n"
    "s_cmp_lt_i32 s96, 0\n"
    "s_cbranch_scc1 Lsp12_%=\n"
    "s_barrier\n"
    "ds_read_b128 a[48:51], v192 offset:16384\n"
    "ds_read_b128 a[52:55], v193 offset:16384\n"
    "v_mfma_f32_16x16x32_bf16 v[100:103], a[112:115], v[172:175], v[100:103]\n"
    "s_add_u32 s44, s44, 33554432\n"
    "s_addc_u32 s45, s45, 0\n"
    "ds_read_b128 v[232:235], v214 offset:8192\n"
    "v_exp_f32 v144, v144\n"
    "v_mfma_f32_16x16x32_bf16 v[104:107], a[112:115], v[176:179], v[104:107]\n"
    "s_cmp_lt_u32 s63, 3\n"
    "s_cselect_b32 s46, 131072, 0\n"
    "ds_read_b128 a[56:59], v194 offset:16384\n"
    "ds_read_b128 a[60:63], v195 offset:16384\n"
    "v_mfma_f32_16x16x32_bf16 v[0:3], a[80:83], v[168:171], v[0:3]\n"
    "s_add_u32 m0, s61, 32768\n"
    "v_exp_f32 v145, v145\n"
    "v_mfma_f32_16x16x32_bf16 v[4:7], a[80:83], v[172:175], v[4:7]\n"
    "s_sub_u32 s96, s96, 60\n"
    "buffer_load_dwordx4 v206, s[44:47], s57 offen offset:0 nt lds\n"
    "v_mfma_f32_16x16x32_bf16 v[8:11], a[80:83], v[176:179], v[8:11]\n"
    "s_max_i32 s96, s96, 0\n"
    "v_exp_f32 v146, v146\n"
    "v_mfma_f32_16x16x32_bf16 v[12:15], a[84:87], v[168:171], v[12:15]\n"
    "s_add_u32 s97, s97, s96\n"
    "ds_read_b128 a[64:67], v192 offset:20480\n"
    "ds_read_b128 a[68:71], v193 offset:20480\n"
    "v_mfma_f32_16x16x32_bf16 v[16:19], a[84:87], v[172:175], v[16:19]\n"
    "s_bfe_u32 s93, s79, 0x80008\n"
    "v_exp_f32 v147, v147\n"
    "v_mfma_f32_16x16x32_bf16 v[20:23], a[84:87], v[176:179], v[20:23]\n"
    "s_add_u32 s97, s97, s93\n"
    "ds_read_b128 a[72:75], v194 offset:20480\n"
    "ds_read_b128 a[76:79], v195 offset:20480\n"
    "v_mfma_f32_16x16x32_bf16 v[24:27], a[88:91], v[168:171], v[24:27]\n"
    "v_exp_f32 v148, v148\n"
    "s_waitcnt lgkmcnt(6)\n"
    "v_lshlrev_b32 v240, 16, v232\n"
    "v_mfma_f32_16x16x32_bf16 v[28:31], a[88:91], v[172:175], v[28:31]\n"
    "buffer_store_dwordx4 a[116:119], v222, s[52:55], s58 offen offset:0 sc0 sc1\n"
    "v_lshlrev_b32 v242, 16, v233\n"
    "v_mfma_f32_16x16x32_bf16 v[32:35], a[88:91], v[176:179], v[32:35]\n"
    "v_exp_f32 v149, v149\n"
    "v_mfma_f32_16x16x32_bf16 v[36:39], a[92:95], v[168:171], v[36:39]\n"
    "ds_read_b64_tr_b16 a[80:81], v196 offset:40960\n"
    "ds_read_b64_tr_b16 a[82:83], v196 offset:45056\n"
    "v_and_b32 v241, s74, v232\n"
    "v_mfma_f32_16x16x32_bf16 v[40:43], a[92:95], v[172:175], v[40:43]\n"
    "v_exp_f32 v150, v150\n"
    "v_mfma_f32_16x16x32_bf16 v[44:47], a[92:95], v[176:179], v[44:47]\n"
    "ds_read_b64_tr_b16 a[84:85], v197 offset:40960\n"
    "ds_read_b64_tr_b16 a[86:87], v197 offset:45056\n"
    "v_and_b32 v243, s74, v233\n"
    "v_mfma_f32_16x16x32_bf16 v[48:51], a[96:99], v[168:171], v[48:51]\n"
    "v_exp_f32 v151, v151\n"
    "v_mfma_f32_16x16x32_bf16 v[52:55], a[96:99], v[172:175], v[52:55]\n"
    "buffer_load_dwordx4 v207, s[44:47], s57 offen offset:1024 nt lds\n"
    "v_mul_f32 v240, s65, v240\n"
    "v_mfma_f32_16x16x32_bf16 v[56:59], a[96:99], v[176:179], v[56:59]\n"
    "s_xor_b32 s57, s57, 8192\n"
    "v_exp_f32 v152, v152\n"
    "v_mul_f32 v242, s65, v242\n"
    "v_mfma_f32_16x16x32_bf16 v[60:63], a[100:103], v[168:171], v[60:63]\n"
    "ds_read_b64_tr_b16 a[88:89], v198 offset:40960\n"
    "ds_read_b64_tr_b16 a[90:91], v198 offset:45056\n"
    "v_mfma_f32_16x16x32_bf16 v[64:67], a[100:103], v[172:175], v[64:67]\n"
    "v_exp_f32 v153, v153\n"
    "v_mul_f32 v241, s65, v241\n"
    "v_mfma_f32_16x16x32_bf16 v[68:71], a[100:103], v[176:179], v[68:71]\n"
    "ds_read_b64_tr_b16 a[92:93], v199 offset:40960\n"
    "ds_read_b64_tr_b16 a[94:95], v199 offset:45056\n"
    "v_mfma_f32_16x16x32_bf16 v[72:75], a[104:107], v[168:171], v[72:75]\n"
    "v_exp_f32 v154, v154\n"
    "v_mul_f32 v243, s65, v243\n"
    "v_mfma_f32_16x16x32_bf16 v[76:79], a[104:107], v[172:175], v[76:79]\n"
    "buffer_store_dwordx4 a[120:123], v222, s[52:55], s58 offen offset:1024 sc0 sc1\n"
    "v_mfma_f32_16x16x32_bf16 v[80:83], a[104:107], v[176:179], v[80:83]\n"
    "s_add_u32 m0, s61, 8192\n"
    "v_exp_f32 v155, v155\n"
    "v_cvt_pk_bf16_f32 v232, v240, v241\n"
    "v_mfma_f32_16x16x32_bf16 v[84:87], a[108:111], v[168:171], v[84:87]\n"
    "ds_read_b64_tr_b16 a[96:97], v200 offset:40960\n"
    "v_cvt_pk_bf16_f32 v180, v144, v145\n"
    "v_cvt_pk_bf16_f32 v233, v242, v243\n"
    "v_mfma_f32_16x16x32_bf16 v[88:91], a[108:111], v[172:175], v[88:91]\n"
    "v_exp_f32 v156, v156\n"
    "v_mfma_f32_16x16x32_bf16 v[92:95], a[108:111], v[176:179], v[92:95]\n"
    "ds_read_b64_tr_b16 a[98:99], v200 offset:45056\n"
    "v_cvt_pk_bf16_f32 v181, v146, v147\n"
    "v_lshlrev_b32 v244, 16, v234\n"
    "v_mfma_f32_16x16x32_bf16 v[120:123], a[48:51], a[0:3], v[108:111]\n"
    "v_exp_f32 v157, v157\n"
    "v_mfma_f32_16x16x32_bf16 v[120:123], a[52:55], a[4:7], v[120:123]\n"
    "buffer_load_dwordx4 v204, s[40:43], s56 offen offset:0 nt lds\n"
    "v_lshlrev_b32 v246, 16, v235\n"
    "s_waitcnt lgkmcnt(2)\n"
    "v_mfma_f32_16x16x32_bf16 v[120:123], a[56:59], a[8:11], v[120:123]\n"
    "v_exp_f32 v158, v158\n"
    "v_mfma_f32_16x16x32_bf16 v[120:123], a[60:63], a[12:15], v[120:123]\n"
    "ds_read_b64_tr_b16 a[100:101], v201 offset:40960\n"
    "v_cvt_pk_bf16_f32 v182, v148, v149\n"
    "v_and_b32 v245, s74, v234\n"
    "v_mfma_f32_16x16x32_bf16 v[124:127], a[64:67], a[0:3], v[108:111]\n"
    "v_exp_f32 v159, v159\n"
    "v_and_b32 v247, s74, v235\n"
    "v_mfma_f32_16x16x32_bf16 v[124:127], a[68:71], a[4:7], v[124:127]\n"
    "ds_read_b64_tr_b16 a[102:103], v201 offset:45056\n"
    "v_cvt_pk_bf16_f32 v183, v150, v151\n"
    "v_mfma_f32_16x16x32_bf16 v[124:127], a[72:75], a[8:11], v[124:127]\n"
    "v_exp_f32 v160, v160\n"
    "v_mul_f32 v244, s65, v244\n"
    "v_mfma_f32_16x16x32_bf16 v[124:127], a[76:79], a[12:15], v[124:127]\n"
    "buffer_store_dwordx4 a[124:127], v222, s[52:55], s58 offen offset:2048 sc0 sc1\n"
    "v_mfma_f32_16x16x32_bf16 v[128:131], a[48:51], a[16:19], v[112:115]\n"
    "v_exp_f32 v161, v161\n"
    "v_mul_f32 v246, s65, v246\n"
    "v_mfma_f32_16x16x32_bf16 v[128:131], a[52:55], a[20:23], v[128:131]\n"
    "ds_read_b64_tr_b16 a[104:105], v202 offset:40960\n"
    "v_cvt_pk_bf16_f32 v184, v152, v153\n"
    "v_mfma_f32_16x16x32_bf16 v[128:131], a[56:59], a[24:27], v[128:131]\n"
    "v_exp_f32 v162, v162\n"
    "v_mul_f32 v245, s65, v245\n"
    "v_mfma_f32_16x16x32_bf16 v[128:131], a[60:63], a[28:31], v[128:131]\n"
    "ds_read_b64_tr_b16 a[106:107], v202 offset:45056\n"
    "v_cvt_pk_bf16_f32 v185, v154, v155\n"
    "v_mul_f32 v247, s65, v247\n"
    "v_mfma_f32_16x16x32_bf16 v[132:135], a[64:67], a[16:19], v[112:115]\n"
    "v_exp_f32 v163, v163\n"
    "v_mfma_f32_16x16x32_bf16 v[132:135], a[68:71], a[20:23], v[132:135]\n"
    "buffer_load_dwordx4 v205, s[40:43], s56 offen offset:1024 nt lds\n"
    "v_cvt_pk_bf16_f32 v234, v244, v245\n"
    "v_mfma_f32_16x16x32_bf16 v[132:135], a[72:75], a[24:27], v[132:135]\n"
    "s_xor_b32 s56, s56, 24576\n"
    "v_exp_f32 v164, v164\n"
    "v_mfma_f32_16x16x32_bf16 v[132:135], a[76:79], a[28:31], v[132:135]\n"
    "ds_read_b64_tr_b16 a[108:109], v203 offset:40960\n"
    "v_cvt_pk_bf16_f32 v186, v156, v157\n"
    "v_cvt_pk_bf16_f32 v235, v246, v247\n"
    "v_mfma_f32_16x16x32_bf16 v[136:139], a[48:51], a[32:35], v[116:119]\n"
    "v_exp_f32 v165, v165\n"
    "v_mfma_f32_16x16x32_bf16 v[136:139], a[52:55], a[36:39], v[136:139]\n"
    "ds_read_b64_tr_b16 a[110:111], v203 offset:45056\n"
    "v_cvt_pk_bf16_f32 v187, v158, v159\n"
    "v_mfma_f32_16x16x32_bf16 v[136:139], a[56:59], a[40:43], v[136:139]\n"
    "ds_write_b128 v214, v[232:235] offset:8192\n"
    "v_exp_f32 v166, v166\n"
    "v_mfma_f32_16x16x32_bf16 v[136:139], a[60:63], a[44:47], v[136:139]\n"
    "s_memrealtime s[94:95]\n"
    "buffer_store_dwordx4 a[128:131], v222, s[52:55], s58 offen offset:3072 sc0 sc1\n"
    "v_mfma_f32_16x16x32_bf16 v[140:143], a[64:67], a[32:35], v[116:119]\n"
    "v_exp_f32 v167, v167\n"
    "v_mfma_f32_16x16x32_bf16 v[140:143], a[68:71], a[36:39], v[140:143]\n"
    "v_cvt_pk_bf16_f32 v188, v160, v161\n"
    "v_cvt_pk_bf16_f32 v189, v162, v163\n"
    "v_mfma_f32_16x16x32_bf16 v[140:143], a[72:75], a[40:43], v[140:143]\n"
    "v_cvt_pk_bf16_f32 v190, v164, v165\n"
    "v_cvt_pk_bf16_f32 v191, v166, v167\n"
    "v_mfma_f32_16x16x32_bf16 v[140:143], a[76:79], a[44:47], v[140:143]\n"
    "s_waitcnt vmcnt(12) lgkmcnt(0)\n"
    "v_mfma_f32_16x16x32_bf16 v[96:99], a[112:115], v[180:183], v[96:99]\n"
    "Lpc13_%=:\n"
    "s_sub_u32 s96, s94, s97\n"
    "s_cmp_lt_i32 s96, 0\n"
    "s_cbranch_scc1 Lsp13_%=\n"
    "s_barrier\n"
    "ds_read_b128 a[48:51], v192 offset:24576\n"
    "ds_read_b128 a[52:55], v193 offset:24576\n"
    "v_mfma_f32_16x16x32_bf16 v[100:103], a[112:115], v[184:187], v[100:103]\n"
    "s_add_u32 m0, s61, 40960\n"
    "ds_read_b128 v[236:239], v215 offset:8192\n"
    "v_exp_f32 v120, v120\n"
    "v_mfma_f32_16x16x32_bf16 v[104:107], a[112:115], v[188:191], v[104:107]\n"
    "s_sub_u32 s96, s96, 60\n"
    "ds_read_b128 a[56:59], v194 offset:24576\n"
    "ds_read_b128 a[60:63], v195 offset:24576\n"
    "v_mfma_f32_16x16x32_bf16 v[0:3], a[80:83], v[180:183], v[0:3]\n"
    "s_max_i32 s96, s96, 0\n"
    "v_exp_f32 v121, v121\n"
    "v_mfma_f32_16x16x32_bf16 v[4:7], a[80:83], v[184:187], v[4:7]\n"
    "s_add_u32 s97, s97, s96\n"
    "buffer_load_dwordx4 v206, s[44:47], s57 offen offset:0 nt lds\n"
    "v_mfma_f32_16x16x32_bf16 v[8:11], a[80:83], v[188:191], v[8:11]\n"
    "s_bfe_u32 s93, s79, 0x80010\n"
    "v_exp_f32 v122, v122\n"
    "v_mfma_f32_16x16x32_bf16 v[12:15], a[84:87], v[180:183], v[12:15]\n"
    "s_add_u32 s97, s97, s93\n"
    "ds_read_b128 a[64:67], v192 offset:28672\n"
    "ds_read_b128 a[68:71], v193 offset:28672\n"
    "v_mfma_f32_16x16x32_bf16 v[16:19], a[84:87], v[184:187], v[16:19]\n"
    "v_exp_f32 v123, v123\n"
    "v_mfma_f32_16x16x32_bf16 v[20:23], a[84:87], v[188:191], v[20:23]\n"
    "ds_read_b128 a[72:75], v194 offset:28672\n"
    "ds_read_b128 a[76:79], v195 offset:28672\n"
    "v_mfma_f32_16x16x32_bf16 v[24:27], a[88:91], v[180:183], v[24:27]\n"
    "v_exp_f32 v124, v124\n"
    "s_waitcnt lgkmcnt(6)\n"
    "v_lshlrev_b32 v240, 16, v236\n"
    "v_mfma_f32_16x16x32_bf16 v[28:31], a[88:91], v[184:187], v[28:31]\n"
    "buffer_store_dwordx4 a[132:135], v222, s[52:55], s59 offen offset:0 sc0 sc1\n"
    "v_lshlrev_b32 v242, 16, v237\n"
    "v_mfma_f32_16x16x32_bf16 v[32:35], a[88:91], v[188:191], v[32:35]\n"
    "v_exp_f32 v125, v125\n"
    "v_mfma_f32_16x16x32_bf16 v[36:39], a[92:95], v[180:183], v[36:39]\n"
    "ds_read_b64_tr_b16 a[80:81], v196 offset:49152\n"
    "ds_read_b64_tr_b16 a[82:83], v196 offset:53248\n"
    "v_and_b32 v241, s74, v236\n"
    "v_mfma_f32_16x16x32_bf16 v[40:43], a[92:95], v[184:187], v[40:43]\n"
    "v_exp_f32 v126, v126\n"
    "v_mfma_f32_16x16x32_bf16 v[44:47], a[92:95], v[188:191], v[44:47]\n"
    "ds_read_b64_tr_b16 a[84:85], v197 offset:49152\n"
    "ds_read_b64_tr_b16 a[86:87], v197 offset:53248\n"
    "v_and_b32 v243, s74, v237\n"
    "v_mfma_f32_16x16x32_bf16 v[48:51], a[96:99], v[180:183], v[48:51]\n"
    "v_exp_f32 v127, v127\n"
    "v_mfma_f32_16x16x32_bf16 v[52:55], a[96:99], v[184:187], v[52:55]\n"
    "buffer_load_dwordx4 v207, s[44:47], s57 offen offset:1024 nt lds\n"
    "v_mul_f32 v240, s65, v240\n"
    "v_mfma_f32_16x16x32_bf16 v[56:59], a[96:99], v[188:191], v[56:59]\n"
    "s_xor_b32 s57, s57, 24576\n"
    "v_exp_f32 v128, v128\n"
    "v_mul_f32 v242, s65, v242\n"
    "v_mfma_f32_16x16x32_bf16 v[60:63], a[100:103], v[180:183], v[60:63]\n"
    "ds_read_b64_tr_b16 a[88:89], v198 offset:49152\n"
    "ds_read_b64_tr_b16 a[90:91], v198 offset:53248\n"
    "v_mfma_f32_16x16x32_bf16 v[64:67], a[100:103], v[184:187], v[64:67]\n"
    "v_exp_f32 v129, v129\n"
    "v_mul_f32 v241, s65, v241\n"
    "v_mfma_f32_16x16x32_bf16 v[68:71], a[100:103], v[188:191], v[68:71]\n"
    "ds_read_b64_tr_b16 a[92:93], v199 offset:49152\n"
    "ds_read_b64_tr_b16 a[94:95], v199 offset:53248\n"
    "v_mfma_f32_16x16x32_bf16 v[72:75], a[104:107], v[180:183], v[72:75]\n"
    "v_exp_f32 v130, v130\n"
    "v_mul_f32 v243, s65, v243\n"
    "v_mfma_f32_16x16x32_bf16 v[76:79], a[104:107], v[184:187], v[76:79]\n"
    "buffer_store_dwordx4 a[136:139], v222, s[52:55], s59 offen offset:1024 sc0 sc1\n"
    "v_mfma_f32_16x16x32_bf16 v[80:83], a[104:107], v[188:191], v[80:83]\n"
    "s_add_u32 m0, s61, 16384\n"
    "v_exp_f32 v131, v131\n"
    "v_cvt_pk_bf16_f32 v236, v240, v241\n"
    "v_mfma_f32_16x16x32_bf16 v[84:87], a[108:111], v[180:183], v[84:87]\n"
    "ds_read_b64_tr_b16 a[96:97], v200 offset:49152\n"
    "v_cvt_pk_bf16_f32 v168, v120, v121\n"
    "v_cvt_pk_bf16_f32 v237, v242, v243\n"
    "v_mfma_f32_16x16x32_bf16 v[88:91], a[108:111], v[184:187], v[88:91]\n"
    "v_exp_f32 v132, v132\n"
    "v_mfma_f32_16x16x32_bf16 v[92:95], a[108:111], v[188:191], v[92:95]\n"
    "ds_read_b64_tr_b16 a[98:99], v200 offset:53248\n"
    "v_cvt_pk_bf16_f32 v169, v122, v123\n"
    "v_lshlrev_b32 v244, 16, v238\n"
    "v_mfma_f32_16x16x32_bf16 v[144:147], a[48:51], a[0:3], v[108:111]\n"
    "v_exp_f32 v133, v133\n"
    "v_mfma_f32_16x16x32_bf16 v[144:147], a[52:55], a[4:7], v[144:147]\n"
    "buffer_load_dwordx4 v204, s[40:43], s56 offen offset:0 nt lds\n"
    "v_lshlrev_b32 v246, 16, v239\n"
    "s_waitcnt lgkmcnt(2)\n"
    "v_mfma_f32_16x16x32_bf16 v[144:147], a[56:59], a[8:11], v[144:147]\n"
    "v_exp_f32 v134, v134\n"
    "v_mfma_f32_16x16x32_bf16 v[144:147], a[60:63], a[12:15], v[144:147]\n"
    "ds_read_b64_tr_b16 a[100:101], v201 offset:49152\n"
    "v_cvt_pk_bf16_f32 v170, v124, v125\n"
    "v_and_b32 v245, s74, v238\n"
    "v_mfma_f32_16x16x32_bf16 v[148:151], a[64:67], a[0:3], v[108:111]\n"
    "v_exp_f32 v135, v135\n"
    "v_and_b32 v247, s74, v239\n"
    "v_mfma_f32_16x16x32_bf16 v[148:151], a[68:71], a[4:7], v[148:151]\n"
    "ds_read_b64_tr_b16 a[102:103], v201 offset:53248\n"
    "v_cvt_pk_bf16_f32 v171, v126, v127\n"
    "v_mfma_f32_16x16x32_bf16 v[148:151], a[72:75], a[8:11], v[148:151]\n"
    "v_exp_f32 v136, v136\n"
    "v_mul_f32 v244, s65, v244\n"
    "v_mfma_f32_16x16x32_bf16 v[148:151], a[76:79], a[12:15], v[148:151]\n"
    "buffer_store_dwordx4 a[140:143], v222, s[52:55], s59 offen offset:2048 sc0 sc1\n"
    "v_mfma_f32_16x16x32_bf16 v[152:155], a[48:51], a[16:19], v[112:115]\n"
    "v_exp_f32 v137, v137\n"
    "v_mul_f32 v246, s65, v246\n"
    "v_mfma_f32_16x16x32_bf16 v[152:155], a[52:55], a[20:23], v[152:155]\n"
    "ds_read_b64_tr_b16 a[104:105], v202 offset:49152\n"
    "v_cvt_pk_bf16_f32 v172, v128, v129\n"
    "v_mfma_f32_16x16x32_bf16 v[152:155], a[56:59], a[24:27], v[152:155]\n"
    "v_exp_f32 v138, v138\n"
    "v_mul_f32 v245, s65, v245\n"
    "v_mfma_f32_16x16x32_bf16 v[152:155], a[60:63], a[28:31], v[152:155]\n"
    "ds_read_b64_tr_b16 a[106:107], v202 offset:53248\n"
    "v_cvt_pk_bf16_f32 v173, v130, v131\n"
    "v_mul_f32 v247, s65, v247\n"
    "v_mfma_f32_16x16x32_bf16 v[156:159], a[64:67], a[16:19], v[112:115]\n"
    "v_exp_f32 v139, v139\n"
    "v_mfma_f32_16x16x32_bf16 v[156:159], a[68:71], a[20:23], v[156:159]\n"
    "buffer_load_dwordx4 v205, s[40:43], s56 offen offset:1024 nt lds\n"
    "v_cvt_pk_bf16_f32 v238, v244, v245\n"
    "v_mfma_f32_16x16x32_bf16 v[156:159], a[72:75], a[24:27], v[156:159]\n"
    "s_xor_b32 s56, s56, 8192\n"
    "v_exp_f32 v140, v140\n"
    "v_mfma_f32_16x16x32_bf16 v[156:159], a[76:79], a[28:31], v[156:159]\n"
    "ds_read_b64_tr_b16 a[108:109], v203 offset:49152\n"
    "v_cvt_pk_bf16_f32 v174, v132, v133\n"
    "v_cvt_pk_bf16_f32 v239, v246, v247\n"
    "v_mfma_f32_16x16x32_bf16 v[160:163], a[48:51], a[32:35], v[116:119]\n"
    "v_exp_f32 v141, v141\n"
    "v_mfma_f32_16x16x32_bf16 v[160:163], a[52:55], a[36:39], v[160:163]\n"
    "ds_read_b64_tr_b16 a[110:111], v203 offset:53248\n"
    "v_cvt_pk_bf16_f32 v175, v134, v135\n"
    "v_mfma_f32_16x16x32_bf16 v[160:163], a[56:59], a[40:43], v[160:163]\n"
    "ds_write_b128 v215, v[236:239] offset:8192\n"
    "v_exp_f32 v142, v142\n"
    "v_mfma_f32_16x16x32_bf16 v[160:163], a[60:63], a[44:47], v[160:163]\n"
    "s_memrealtime s[94:95]\n"
    "buffer_store_dwordx4 a[144:147], v222, s[52:55], s59 offen offset:3072 sc0 sc1\n"
    "v_mfma_f32_16x16x32_bf16 v[164:167], a[64:67], a[32:35], v[116:119]\n"
    "v_exp_f32 v143, v143\n"
    "v_mfma_f32_16x16x32_bf16 v[164:167], a[68:71], a[36:39], v[164:167]\n"
    "v_cvt_pk_bf16_f32 v176, v136, v137\n"
    "v_cvt_pk_bf16_f32 v177, v138, v139\n"
    "v_mfma_f32_16x16x32_bf16 v[164:167], a[72:75], a[40:43], v[164:167]\n"
    "v_cvt_pk_bf16_f32 v178, v140, v141\n"
    "v_cvt_pk_bf16_f32 v179, v142, v143\n"
    "v_mfma_f32_16x16x32_bf16 v[164:167], a[76:79], a[44:47], v[164:167]\n"
    "s_cmp_eq_u32 s63, 3\n"
    "s_cbranch_scc1 Ldrain_%=\n"
    "s_waitcnt vmcnt(16) lgkmcnt(0)\n"
    "v_mfma_f32_16x16x32_bf16 v[96:99], a[112:115], v[168:171], v[96:99]\n"
    "Lpc14_%=:\n"
    "s_sub_u32 s96, s94, s97\n"
    "s_cmp_lt_i32 s96, 0\n"
    "s_cbranch_scc1 Lsp14_%=\n"
    "s_barrier\n"
    "ds_read_b128 a[48:51], v192 offset:0\n"
    "ds_read_b128 a[52:55], v193 offset:0\n"
    "v_mfma_f32_16x16x32_bf16 v[100:103], a[112:115], v[172:175], v[100:103]\n"
    "s_add_u32 m0, s61, 49152\n"
    "ds_read_b128 a[0:3], v212 offset:0\n"
    "v_exp_f32 v144, v144\n"
    "v_mfma_f32_16x16x32_bf16 v[104:107], a[112:115], v[176:179], v[104:107]\n"
    "s_sub_u32 s96, s96, 60\n"
    "ds_read_b128 a[56:59], v194 offset:0\n"
    "ds_read_b128 a[60:63], v195 offset:0\n"
    "v_mfma_f32_16x16x32_bf16 v[0:3], a[80:83], v[168:171], v[0:3]\n"
    "s_max_i32 s96, s96, 0\n"
    "ds_read_b128 a[4:7], v213 offset:0\n"
    "v_exp_f32 v145, v145\n"
    "v_mfma_f32_16x16x32_bf16 v[4:7], a[80:83], v[172:175], v[4:7]\n"
    "s_add_u32 s97, s97, s96\n"
    "buffer_load_dwordx4 v206, s[44:47], s57 offen offset:0 nt lds\n"
    "v_mfma_f32_16x16x32_bf16 v[8:11], a[80:83], v[176:179], v[8:11]\n"
    "s_bfe_u32 s93, s79, 0x80018\n"
    "ds_read_b128 a[8:11], v214 offset:0\n"
    "v_exp_f32 v146, v146\n"
    "v_mfma_f32_16x16x32_bf16 v[12:15], a[84:87], v[168:171], v[12:15]\n"
    "s_add_u32 s97, s97, s93\n"
    "ds_read_b128 a[64:67], v192 offset:4096\n"
    "ds_read_b128 a[68:71], v193 offset:4096\n"
    "v_mfma_f32_16x16x32_bf16 v[16:19], a[84:87], v[172:175], v[16:19]\n"
    "ds_read_b128 a[12:15], v215 offset:0\n"
    "v_exp_f32 v147, v147\n"
    "v_mfma_f32_16x16x32_bf16 v[20:23], a[84:87], v[176:179], v[20:23]\n"
    "ds_read_b128 a[72:75], v194 offset:4096\n"
    "ds_read_b128 a[76:79], v195 offset:4096\n"
    "v_mfma_f32_16x16x32_bf16 v[24:27], a[88:91], v[168:171], v[24:27]\n"
    "ds_read_b128 a[16:19], v212 offset:4096\n"
    "v_exp_f32 v148, v148\n"
    "v_mfma_f32_16x16x32_bf16 v[28:31], a[88:91], v[172:175], v[28:31]\n"
    "buffer_store_dwordx4 a[148:151], v222, s[52:55], s60 offen offset:0 sc0 sc1\n"
    "v_mfma_f32_16x16x32_bf16 v[32:35], a[88:91], v[176:179], v[32:35]\n"
    "ds_read_b128 a[20:23], v213 offset:4096\n"
    "v_exp_f32 v149, v149\n"
    "v_mfma_f32_16x16x32_bf16 v[36:39], a[92:95], v[168:171], v[36:39]\n"
    "ds_read_b64_tr_b16 a[80:81], v196 offset:57344\n"
    "ds_read_b64_tr_b16 a[82:83], v196 offset:61440\n"
    "v_mfma_f32_16x16x32_bf16 v[40:43], a[92:95], v[172:175], v[40:43]\n"
    "ds_read_b128 a[24:27], v214 offset:4096\n"
    "v_exp_f32 v150, v150\n"
    "v_mfma_f32_16x16x32_bf16 v[44:47], a[92:95], v[176:179], v[44:47]\n"
    "ds_read_b64_tr_b16 a[84:85], v197 offset:57344\n"
    "ds_read_b64_tr_b16 a[86:87], v197 offset:61440\n"
    "v_mfma_f32_16x16x32_bf16 v[48:51], a[96:99], v[168:171], v[48:51]\n"
    "ds_read_b128 a[28:31], v215 offset:4096\n"
    "v_exp_f32 v151, v151\n"
    "v_mfma_f32_16x16x32_bf16 v[52:55], a[96:99], v[172:175], v[52:55]\n"
    "buffer_load_dwordx4 v207, s[44:47], s57 offen offset:1024 nt lds\n"
    "v_mfma_f32_16x16x32_bf16 v[56:59], a[96:99], v[176:179], v[56:59]\n"
    "s_xor_b32 s57, s57, 8192\n"
    "ds_read_b128 a[32:35], v212 offset:8192\n"
    "v_exp_f32 v152, v152\n"
    "v_mfma_f32_16x16x32_bf16 v[60:63], a[100:103], v[168:171], v[60:63]\n"
    "ds_read_b64_tr_b16 a[88:89], v198 offset:57344\n"
    "ds_read_b64_tr_b16 a[90:91], v198 offset:61440\n"
    "v_mfma_f32_16x16x32_bf16 v[64:67], a[100:103], v[172:175], v[64:67]\n"
    "ds_read_b128 a[36:39], v213 offset:8192\n"
    "v_exp_f32 v153, v153\n"
    "v_mfma_f32_16x16x32_bf16 v[68:71], a[100:103], v[176:179], v[68:71]\n"
    "ds_read_b64_tr_b16 a[92:93], v199 offset:57344\n"
    "ds_read_b64_tr_b16 a[94:95], v199 offset:61440\n"
    "v_mfma_f32_16x16x32_bf16 v[72:75], a[104:107], v[168:171], v[72:75]\n"
    "ds_read_b128 a[40:43], v214 offset:8192\n"
    "v_exp_f32 v154, v154\n"
    "v_mfma_f32_16x16x32_bf16 v[76:79], a[104:107], v[172:175], v[76:79]\n"
    "buffer_store_dwordx4 a[152:155], v222, s[52:55], s60 offen offset:1024 sc0 sc1\n"
    "v_mfma_f32_16x16x32_bf16 v[80:83], a[104:107], v[176:179], v[80:83]\n"
    "s_add_u32 m0, s61, 24576\n"
    "ds_read_b128 a[44:47], v215 offset:8192\n"
    "v_exp_f32 v155, v155\n"
    "v_mfma_f32_16x16x32_bf16 v[84:87], a[108:111], v[168:171], v[84:87]\n"
    "ds_read_b64_tr_b16 a[96:97], v200 offset:57344\n"
    "v_cvt_pk_bf16_f32 v180, v144, v145\n"
    "v_mfma_f32_16x16x32_bf16 v[88:91], a[108:111], v[172:175], v[88:91]\n"
    "v_exp_f32 v156, v156\n"
    "v_mfma_f32_16x16x32_bf16 v[92:95], a[108:111], v[176:179], v[92:95]\n"
    "ds_read_b64_tr_b16 a[98:99], v200 offset:61440\n"
    "v_cvt_pk_bf16_f32 v181, v146, v147\n"
    "s_waitcnt lgkmcnt(8)\n"
    "v_mfma_f32_16x16x32_bf16 v[120:123], a[48:51], a[0:3], 0\n"
    "v_exp_f32 v157, v157\n"
    "v_mfma_f32_16x16x32_bf16 v[120:123], a[52:55], a[4:7], v[120:123]\n"
    "buffer_load_dwordx4 v204, s[40:43], s56 offen offset:0 nt lds\n"
    "v_mfma_f32_16x16x32_bf16 v[120:123], a[56:59], a[8:11], v[120:123]\n"
    "v_exp_f32 v158, v158\n"
    "v_mfma_f32_16x16x32_bf16 v[120:123], a[60:63], a[12:15], v[120:123]\n"
    "ds_read_b64_tr_b16 a[100:101], v201 offset:57344\n"
    "v_cvt_pk_bf16_f32 v182, v148, v149\n"
    "v_mfma_f32_16x16x32_bf16 v[124:127], a[64:67], a[0:3], 0\n"
    "v_exp_f32 v159, v159\n"
    "v_mfma_f32_16x16x32_bf16 v[124:127], a[68:71], a[4:7], v[124:127]\n"
    "ds_read_b64_tr_b16 a[102:103], v201 offset:61440\n"
    "v_cvt_pk_bf16_f32 v183, v150, v151\n"
    "v_mfma_f32_16x16x32_bf16 v[124:127], a[72:75], a[8:11], v[124:127]\n"
    "v_exp_f32 v160, v160\n"
    "v_mfma_f32_16x16x32_bf16 v[124:127], a[76:79], a[12:15], v[124:127]\n"
    "buffer_store_dwordx4 a[156:159], v222, s[52:55], s60 offen offset:2048 sc0 sc1\n"
    "v_mfma_f32_16x16x32_bf16 v[128:131], a[48:51], a[16:19], 0\n"
    "v_exp_f32 v161, v161\n"
    "v_mfma_f32_16x16x32_bf16 v[128:131], a[52:55], a[20:23], v[128:131]\n"
    "ds_read_b64_tr_b16 a[104:105], v202 offset:57344\n"
    "v_cvt_pk_bf16_f32 v184, v152, v153\n"
    "v_mfma_f32_16x16x32_bf16 v[128:131], a[56:59], a[24:27], v[128:131]\n"
    "v_exp_f32 v162, v162\n"
    "v_mfma_f32_16x16x32_bf16 v[128:131], a[60:63], a[28:31], v[128:131]\n"
    "ds_read_b64_tr_b16 a[106:107], v202 offset:61440\n"
    "v_cvt_pk_bf16_f32 v185, v154, v155\n"
    "v_mfma_f32_16x16x32_bf16 v[132:135], a[64:67], a[16:19], 0\n"
    "v_exp_f32 v163, v163\n"
    "v_mfma_f32_16x16x32_bf16 v[132:135], a[68:71], a[20:23], v[132:135]\n"
    "buffer_load_dwordx4 v205, s[40:43], s56 offen offset:1024 nt lds\n"
    "v_mfma_f32_16x16x32_bf16 v[132:135], a[72:75], a[24:27], v[132:135]\n"
    "s_xor_b32 s56, s56, 57344\n"
    "v_exp_f32 v164, v164\n"
    "v_mfma_f32_16x16x32_bf16 v[132:135], a[76:79], a[28:31], v[132:135]\n"
    "ds_read_b64_tr_b16 a[108:109], v203 offset:57344\n"
    "v_cvt_pk_bf16_f32 v186, v156, v157\n"
    "v_mfma_f32_16x16x32_bf16 v[136:139], a[48:51], a[32:35], 0\n"
    "v_exp_f32 v165, v165\n"
    "s_waitcnt lgkmcnt(3)\n"
    "v_mfma_f32_16x16x32_bf16 v[136:139], a[52:55], a[36:39], v[136:139]\n"
    "ds_read_b64_tr_b16 a[110:111], v203 offset:61440\n"
    "v_cvt_pk_bf16_f32 v187, v158, v159\n"
    "v_mfma_f32_16x16x32_bf16 v[136:139], a[56:59], a[40:43], v[136:139]\n"
    "v_exp_f32 v166, v166\n"
    "v_mfma_f32_16x16x32_bf16 v[136:139], a[60:63], a[44:47], v[136:139]\n"
    "s_memrealtime s[94:95]\n"
    "buffer_store_dwordx4 a[160:163], v222, s[52:55], s60 offen offset:3072 sc0 sc1\n"
    "v_mfma_f32_16x16x32_bf16 v[140:143], a[64:67], a[32:35], 0\n"
    "s_add_u32 s52, s52, 12582912\n"
    "s_addc_u32 s53, s53, 0\n"
    "v_exp_f32 v167, v167\n"
    "v_mfma_f32_16x16x32_bf16 v[140:143], a[68:71], a[36:39], v[140:143]\n"
    "s_mov_b32 s54, 49152\n"
    "v_cvt_pk_bf16_f32 v188, v160, v161\n"
    "v_cvt_pk_bf16_f32 v189, v162, v163\n"
    "v_mfma_f32_16x16x32_bf16 v[140:143], a[72:75], a[40:43], v[140:143]\n"
    "v_cvt_pk_bf16_f32 v190, v164, v165\n"
    "v_cvt_pk_bf16_f32 v191, v166, v167\n"
    "v_mfma_f32_16x16x32_bf16 v[140:143], a[76:79], a[44:47], v[140:143]\n"
    "s_waitcnt vmcnt(17) lgkmcnt(0)\n"
    "v_mfma_f32_16x16x32_bf16 v[96:99], a[112:115], v[180:183], v[96:99]\n"
    "Lpc15_%=:\n"
    "s_sub_u32 s96, s94, s97\n"
    "s_cmp_lt_i32 s96, 0\n"
    "s_cbranch_scc1 Lsp15_%=\n"
    "s_barrier\n"
    "ds_read_b128 a[48:51], v192 offset:8192\n"
    "v_max3_f32 v250, v120, v121, v122\n"
    "v_max3_f32 v252, v128, v129, v130\n"
    "v_max3_f32 v254, v136, v137, v138\n"
    "v_mfma_f32_16x16x32_bf16 v[100:103], a[112:115], v[184:187], v[100:103]\n"
    "s_add_u32 s63, s63, 1\n"
    "ds_read_b128 a[52:55], v193 offset:8192\n"
    "v_max3_f32 v251, v123, v124, v125\n"
    "v_max3_f32 v253, v131, v132, v133\n"
    "v_max3_f32 v255, v139, v140, v141\n"
    "v_mfma_f32_16x16x32_bf16 v[104:107], a[112:115], v[188:191], v[104:107]\n"
    "s_add_u32 m0, s61, 57344\n"
    "v_max3_f32 v250, v250, v126, v127\n"
    "v_max3_f32 v252, v252, v134, v135\n"
    "v_mfma_f32_16x16x32_bf16 v[0:3], a[80:83], v[180:183], v[0:3]\n"
    "s_sub_u32 s96, s96, 60\n"
    "ds_read_b128 a[56:59], v194 offset:8192\n"
    "v_max3_f32 v254, v254, v142, v143\n"
    "v_max_f32 v250, v250, v251\n"
    "v_max_f32 v252, v252, v253\n"
    "v_mfma_f32_16x16x32_bf16 v[4:7], a[80:83], v[184:187], v[4:7]\n"
    "s_max_i32 s96, s96, 0\n"
    "ds_read_b128 a[60:63], v195 offset:8192\n"
    "v_max_f32 v254, v254, v255\n"
    "v_mov_b32 v251, v250\n"
    "v_mov_b32 v253, v252\n"
    "v_mfma_f32_16x16x32_bf16 v[8:11], a[80:83], v[188:191], v[8:11]\n"
    "s_add_u32 s97, s97, s96\n"
    "v_mov_b32 v255, v254\n"
    "v_permlane16_swap_b32 v250, v251\n"
    "v_mfma_f32_16x16x32_bf16 v[12:15], a[84:87], v[180:183], v[12:15]\n"
    "s_mov_b64 s[76:77], s[80:81]\n"
    "ds_read_b128 a[64:67], v192 offset:12288\n"
    "v_permlane16_swap_b32 v252, v253\n"
    "v_permlane16_swap_b32 v254, v255\n"
    "v_max_f32 v250, v250, v251\n"
    "v_mfma_f32_16x16x32_bf16 v[16:19], a[84:87], v[184:187], v[16:19]\n"
    "s_mov_b64 s[78:79], s[82:83]\n"
    "buffer_load_dwordx4 v206, s[44:47], s57 offen offset:0 nt lds\n"
    "v_max_f32 v252, v252, v253\n"
    "v_max_f32 v254, v254, v255\n"
    "v_mfma_f32_16x16x32_bf16 v[20:23], a[84:87], v[188:191], v[20:23]\n"
    "s_mov_b64 s[80:81], s[84:85]\n"
    "v_mov_b32 v251, v250\n"
    "v_mov_b32 v253, v252\n"
    "v_mov_b32 v255, v254\n"
    "v_mfma_f32_16x16x32_bf16 v[24:27], a[88:91], v[180:183], v[24:27]\n"
    "s_mov_b64 s[82:83], s[86:87]\n"
    "ds_read_b128 a[68:71], v193 offset:12288\n"
    "v_permlane32_swap_b32 v250, v251\n"
    "v_permlane32_swap_b32 v252, v253\n"
    "v_permlane32_swap_b32 v254, v255\n"
    "v_mfma_f32_16x16x32_bf16 v[28:31], a[88:91], v[184:187], v[28:31]\n"
    "s_mov_b64 s[84:85], s[88:89]\n"
    "ds_read_b128 a[72:75], v194 offset:12288\n"
    "v_max_f32 v250, v250, v251\n"
    "v_max_f32 v252, v252, v253\n"
    "v_rcp_f32 v247, v96\n"
    "v_mfma_f32_16x16x32_bf16 v[32:35], a[88:91], v[188:191], v[32:35]\n"
    "s_mov_b64 s[86:87], s[90:91]\n"
    "v_max_f32 v254, v254, v255\n"
    "v_sub_f32 v108, 0, v250\n"
    "v_sub_f32 v112, 0, v252\n"
    "v_rcp_f32 v248, v100\n"
    "v_mfma_f32_16x16x32_bf16 v[36:39], a[92:95], v[180:183], v[36:39]\n"
    "s_bfe_u32 s93, s76, 0x80000\n"
    "ds_read_b128 a[76:79], v195 offset:12288\n"
    "v_sub_f32 v116, 0, v254\n"
    "v_sub_f32 v109, 0, v250\n"
    "v_sub_f32 v113, 0, v252\n"
    "v_rcp_f32 v249, v104\n"
    "v_mfma_f32_16x16x32_bf16 v[40:43], a[92:95], v[184:187], v[40:43]\n"
    "s_add_u32 s97, s97, s93\n"
    "v_sub_f32 v117, 0, v254\n"
    "v_sub_f32 v110, 0, v250\n"
    "v_cmp_nge_f32 s[68:69], s66, v96\n"
    "v_mfma_f32_16x16x32_bf16 v[44:47], a[92:95], v[188:191], v[44:47]\n"
    "ds_read_b64_tr_b16 a[80:81], v196 offset:32768\n"
    "v_sub_f32 v114, 0, v252\n"
    "v_sub_f32 v118, 0, v254\n"
    "v_sub_f32 v111, 0, v250\n"
    "v_cmp_nge_f32 s[70:71], s66, v100\n"
    "v_mfma_f32_16x16x32_bf16 v[48:51], a[96:99], v[180:183], v[48:51]\n"
    "buffer_load_dwordx4 v207, s[44:47], s57 offen offset:1024 nt lds\n"
    "v_sub_f32 v115, 0, v252\n"
    "v_sub_f32 v119, 0, v254\n"
    "v_cmp_nge_f32 s[72:73], s66, v104\n"
    "v_mfma_f32_16x16x32_bf16 v[52:55], a[96:99], v[184:187], v[52:55]\n"
    "s_xor_b32 s57, s57, 57344\n"
    "ds_read_b64_tr_b16 a[82:83], v196 offset:36864\n"
    "v_sub_f32 v120, v120, v250\n"
    "v_mfma_f32_16x16x32_bf16 v[56:59], a[96:99], v[188:191], v[56:59]\n"
    "s_add_u32 s48, s48, 12582912\n"
    "s_addc_u32 s49, s49, 0\n"
    "v_sub_f32 v121, v121, v250\n"
    "v_exp_f32 v120, v120\n"
    "v_mfma_f32_16x16x32_bf16 v[60:63], a[100:103], v[180:183], v[60:63]\n"
    "s_cmp_lt_u32 s63, 3\n"
    "s_cselect_b32 s50, 49152, 0\n"
    "ds_read_b64_tr_b16 a[84:85], v197 offset:32768\n"
    "v_sub_f32 v122, v122, v250\n"
    "v_exp_f32 v121, v121\n"
    "v_mfma_f32_16x16x32_bf16 v[64:67], a[100:103], v[184:187], v[64:67]\n"
    "s_add_u32 m0, s62, 0\n"
    "ds_read_b64_tr_b16 a[86:87], v197 offset:36864\n"
    "v_cvt_pk_bf16_f32 v168, v120, v121\n"
    "v_exp_f32 v122, v122\n"
    "v_mfma_f32_16x16x32_bf16 v[68:71], a[100:103], v[188:191], v[68:71]\n"
    "v_sub_f32 v123, v123, v250\n"
    "v_mfma_f32_16x16x32_bf16 v[72:75], a[104:107], v[180:183], v[72:75]\n"
    "ds_read_b64_tr_b16 a[88:89], v198 offset:32768\n"
    "v_sub_f32 v124, v124, v250\n"
    "v_exp_f32 v123, v123\n"
    "v_mfma_f32_16x16x32_bf16 v[76:79], a[104:107], v[184:187], v[76:79]\n"
    "v_sub_f32 v125, v125, v250\n"
    "v_cvt_pk_bf16_f32 v169, v122, v123\n"
    "v_exp_f32 v124, v124\n"
    "v_mfma_f32_16x16x32_bf16 v[80:83], a[104:107], v[188:191], v[80:83]\n"
    "buffer_load_dwordx4 v208, s[48:51], s58 offen offset:0 nt lds\n"
    "v_exp_f32 v125, v125\n"
    "v_mfma_f32_16x16x32_bf16 v[84:87], a[108:111], v[180:183], v[84:87]\n"
    "s_or_b64 s[68:69], s[68:69], s[70:71]\n"
    "s_lshl_b32 s64, s64, 1\n"
    "s_or_b64 s[68:69], s[68:69], s[72:73]\n"
    "s_cselect_b32 s67, 1, 0\n"
    "s_or_b32 s64, s64, s67\n"
    "ds_read_b64_tr_b16 a[90:91], v198 offset:36864\n"
    "v_sub_f32 v126, v126, v250\n"
    "v_cvt_pk_bf16_f32 v170, v124, v125\n"
    "v_mfma_f32_16x16x32_bf16 v[88:91], a[108:111], v[184:187], v[88:91]\n"
    "s_add_u32 m0, s61, 0\n"
    "ds_read_b64_tr_b16 a[92:93], v199 offset:32768\n"
    "v_sub_f32 v127, v127, v250\n"
    "v_exp_f32 v126, v126\n"
    "v_mfma_f32_16x16x32_bf16 v[92:95], a[108:111], v[188:191], v[92:95]\n"
    "v_sub_f32 v128, v128, v252\n"
    "v_exp_f32 v127, v127\n"
    "v_mul_f32 v0, v247, v0\n"
    "v_mul_f32 v1, v247, v1\n"
    "v_mul_f32 v2, v247, v2\n"
    "s_waitcnt lgkmcnt(2)\n"
    "v_mfma_f32_16x16x32_bf16 v[144:147], a[48:51], a[0:3], v[108:111]\n"
    "ds_read_b64_tr_b16 a[94:95], v199 offset:36864\n"
    "v_cvt_pk_bf16_f32 v171, v126, v127\n"
    "v_exp_f32 v128, v128\n"
    "v_mul_f32 v3, v247, v3\n"
    "v_cvt_pk_bf16_f32 v240, v0, v1\n"
    "v_cvt_pk_bf16_f32 v241, v2, v3\n"
    "v_mfma_f32_16x16x32_bf16 v[144:147], a[52:55], a[4:7], v[144:147]\n"
    "ds_write_b64 v216, v[240:241] offset:0\n"
    "v_sub_f32 v129, v129, v252\n"
    "v_mul_f32 v4, v248, v4\n"
    "v_mul_f32 v5, v248, v5\n"
    "v_mul_f32 v6, v248, v6\n"
    "v_mfma_f32_16x16x32_bf16 v[144:147], a[56:59], a[8:11], v[144:147]\n"
    "ds_read_b64_tr_b16 a[96:97], v200 offset:32768\n"
    "v_sub_f32 v130, v130, v252\n"
    "v_exp_f32 v129, v129\n"
    "v_mul_f32 v7, v248, v7\n"
    "v_cvt_pk_bf16_f32 v242, v4, v5\n"
    "v_cvt_pk_bf16_f32 v243, v6, v7\n"
    "v_mfma_f32_16x16x32_bf16 v[144:147], a[60:63], a[12:15], v[144:147]\n"
    "ds_read_b64_tr_b16 a[98:99], v200 offset:36864\n"
    "ds_write_b64 v216, v[242:243] offset:4096\n"
    "v_sub_f32 v131, v131, v252\n"
    "v_cvt_pk_bf16_f32 v172, v128, v129\n"
    "v_exp_f32 v130, v130\n"
    "v_mul_f32 v8, v249, v8\n"
    "v_mul_f32 v9, v249, v9\n"
    "v_mul_f32 v10, v249, v10\n"
    "v_mfma_f32_16x16x32_bf16 v[148:151], a[64:67], a[0:3], v[108:111]\n"
    "buffer_load_dwordx4 v204, s[40:43], s56 offen offset:0 nt lds\n"
    "v_exp_f32 v131, v131\n"
    "v_mul_f32 v11, v249, v11\n"
    "v_cvt_pk_bf16_f32 v244, v8, v9\n"
    "v_cvt_pk_bf16_f32 v245, v10, v11\n"
    "v_mfma_f32_16x16x32_bf16 v[148:151], a[68:71], a[4:7], v[148:151]\n"
    "ds_write_b64 v216, v[244:245] offset:8192\n"
    "v_sub_f32 v132, v132, v252\n"
    "v_cvt_pk_bf16_f32 v173, v130, v131\n"
    "v_mul_f32 v12, v247, v12\n"
    "v_mul_f32 v13, v247, v13\n"
    "v_mul_f32 v14, v247, v14\n"
    "v_mfma_f32_16x16x32_bf16 v[148:151], a[72:75], a[8:11], v[148:151]\n"
    "ds_read_b64_tr_b16 a[100:101], v201 offset:32768\n"
    "v_sub_f32 v133, v133, v252\n"
    "v_exp_f32 v132, v132\n"
    "v_mul_f32 v15, v247, v15\n"
    "v_cvt_pk_bf16_f32 v240, v12, v13\n"
    "v_cvt_pk_bf16_f32 v241, v14, v15\n"
    "v_mfma_f32_16x16x32_bf16 v[148:151], a[76:79], a[12:15], v[148:151]\n"
    "ds_read_b64_tr_b16 a[102:103], v201 offset:36864\n"
    "ds_write_b64 v217, v[240:241] offset:0\n"
    "v_sub_f32 v134, v134, v252\n"
    "v_exp_f32 v133, v133\n"
    "v_mul_f32 v16, v248, v16\n"
    "v_mul_f32 v17, v248, v17\n"
    "v_mul_f32 v18, v248, v18\n"
    "v_mfma_f32_16x16x32_bf16 v[152:155], a[48:51], a[16:19], v[112:115]\n"
    "v_cvt_pk_bf16_f32 v174, v132, v133\n"
    "v_exp_f32 v134, v134\n"
    "v_mul_f32 v19, v248, v19\n"
    "v_cvt_pk_bf16_f32 v242, v16, v17\n"
    "v_cvt_pk_bf16_f32 v243, v18, v19\n"
    "v_mfma_f32_16x16x32_bf16 v[152:155], a[52:55], a[20:23], v[152:155]\n"
    "ds_read_b64_tr_b16 a[104:105], v202 offset:32768\n"
    "ds_write_b64 v217, v[242:243] offset:4096\n"
    "v_sub_f32 v135, v135, v252\n"
    "v_mul_f32 v20, v249, v20\n"
    "v_mul_f32 v21, v249, v21\n"
    "v_mul_f32 v22, v249, v22\n"
    "v_mfma_f32_16x16x32_bf16 v[152:155], a[56:59], a[24:27], v[152:155]\n"
    "v_sub_f32 v136, v136, v254\n"
    "v_exp_f32 v135, v135\n"
    "v_mul_f32 v23, v249, v23\n"
    "v_cvt_pk_bf16_f32 v244, v20, v21\n"
    "v_cvt_pk_bf16_f32 v245, v22, v23\n"
    "v_mfma_f32_16x16x32_bf16 v[152:155], a[60:63], a[28:31], v[152:155]\n"
    "ds_read_b64_tr_b16 a[106:107], v202 offset:36864\n"
    "ds_write_b64 v217, v[244:245] offset:8192\n"
    "v_sub_f32 v137, v137, v254\n"
    "v_cvt_pk_bf16_f32 v175, v134, v135\n"
    "v_exp_f32 v136, v136\n"
    "v_mul_f32 v24, v247, v24\n"
    "v_mul_f32 v25, v247, v25\n"
    "v_mul_f32 v26, v247, v26\n"
    "v_mfma_f32_16x16x32_bf16 v[156:159], a[64:67], a[16:19], v[112:115]\n"
    "buffer_load_dwordx4 v205, s[40:43], s56 offen offset:1024 nt lds\n"
    "v_exp_f32 v137, v137\n"
    "v_mul_f32 v27, v247, v27\n"
    "v_cvt_pk_bf16_f32 v240, v24, v25\n"
    "v_cvt_pk_bf16_f32 v241, v26, v27\n"
    "v_mfma_f32_16x16x32_bf16 v[156:159], a[68:71], a[20:23], v[156:159]\n"
    "s_xor_b32 s56, s56, 8192\n"
    "ds_read_b64_tr_b16 a[108:109], v203 offset:32768\n"
    "ds_write_b64 v218, v[240:241] offset:0\n"
    "v_sub_f32 v138, v138, v254\n"
    "v_cvt_pk_bf16_f32 v176, v136, v137\n"
    "v_mul_f32 v28, v248, v28\n"
    "v_mul_f32 v29, v248, v29\n"
    "v_mul_f32 v30, v248, v30\n"
    "v_mfma_f32_16x16x32_bf16 v[156:159], a[72:75], a[24:27], v[156:159]\n"
    "s_add_u32 m0, s62, 0\n"
    "v_sub_f32 v139, v139, v254\n"
    "v_exp_f32 v138, v138\n"
    "v_mul_f32 v31, v248, v31\n"
    "v_cvt_pk_bf16_f32 v242, v28, v29\n"
    "v_cvt_pk_bf16_f32 v243, v30, v31\n"
    "v_mfma_f32_16x16x32_bf16 v[156:159], a[76:79], a[28:31], v[156:159]\n"
    "ds_read_b64_tr_b16 a[110:111], v203 offset:36864\n"
    "ds_write_b64 v218, v[242:243] offset:4096\n"
    "v_sub_f32 v140, v140, v254\n"
    "v_exp_f32 v139, v139\n"
    "v_mul_f32 v32, v249, v32\n"
    "v_mul_f32 v33, v249, v33\n"
    "v_mul_f32 v34, v249, v34\n"
    "v_mfma_f32_16x16x32_bf16 v[160:163], a[48:51], a[32:35], v[116:119]\n"
    "v_cvt_pk_bf16_f32 v177, v138, v139\n"
    "v_exp_f32 v140, v140\n"
    "v_mul_f32 v35, v249, v35\n"
    "v_cvt_pk_bf16_f32 v244, v32, v33\n"
    "v_cvt_pk_bf16_f32 v245, v34, v35\n"
    "v_mfma_f32_16x16x32_bf16 v[160:163], a[52:55], a[36:39], v[160:163]\n"
    "ds_write_b64 v218, v[244:245] offset:8192\n"
    "v_sub_f32 v141, v141, v254\n"
    "v_mul_f32 v36, v247, v36\n"
    "v_mul_f32 v37, v247, v37\n"
    "v_mul_f32 v38, v247, v38\n"
    "v_mfma_f32_16x16x32_bf16 v[160:163], a[56:59], a[40:43], v[160:163]\n"
    "v_sub_f32 v142, v142, v254\n"
    "v_exp_f32 v141, v141\n"
    "v_mul_f32 v39, v247, v39\n"
    "v_cvt_pk_bf16_f32 v240, v36, v37\n"
    "v_cvt_pk_bf16_f32 v241, v38, v39\n"
    "v_mfma_f32_16x16x32_bf16 v[160:163], a[60:63], a[44:47], v[160:163]\n"
    "s_memrealtime s[94:95]\n"
    "ds_write_b64 v219, v[240:241] offset:0\n"
    "v_sub_f32 v143, v143, v254\n"
    "v_cvt_pk_bf16_f32 v178, v140, v141\n"
    "v_exp_f32 v142, v142\n"
    "v_mul_f32 v40, v248, v40\n"
    "v_mul_f32 v41, v248, v41\n"
    "v_mul_f32 v42, v248, v42\n"
    "v_mfma_f32_16x16x32_bf16 v[164:167], a[64:67], a[32:35], v[116:119]\n"
    "buffer_load_dwordx4 v209, s[48:51], s58 offen offset:1024 nt lds\n"
    "v_exp_f32 v143, v143\n"
    "v_mul_f32 v43, v248, v43\n"
    "v_cvt_pk_bf16_f32 v242, v40, v41\n"
    "v_cvt_pk_bf16_f32 v243, v42, v43\n"
    "v_mfma_f32_16x16x32_bf16 v[164:167], a[68:71], a[36:39], v[164:167]\n"
    "ds_write_b64 v219, v[242:243] offset:4096\n"
    "v_cvt_pk_bf16_f32 v179, v142, v143\n"
    "v_mul_f32 v44, v249, v44\n"
    "v_mul_f32 v45, v249, v45\n"
    "v_mul_f32 v46, v249, v46\n"
    "v_mfma_f32_16x16x32_bf16 v[164:167], a[72:75], a[40:43], v[164:167]\n"
    "v_mul_f32 v47, v249, v47\n"
    "v_cvt_pk_bf16_f32 v244, v44, v45\n"
    "v_cvt_pk_bf16_f32 v245, v46, v47\n"
    "v_mfma_f32_16x16x32_bf16 v[164:167], a[76:79], a[44:47], v[164:167]\n"
    "ds_write_b64 v219, v[244:245] offset:8192\n"
    "s_waitcnt vmcnt(15) lgkmcnt(0)\n"
    "v_mfma_f32_16x16x32_bf16 v[96:99], a[112:115], v[168:171], 0\n"
    "Lpc16_%=:\n"
    "s_sub_u32 s96, s94, s97\n"
    "s_cmp_lt_i32 s96, 0\n"
    "s_cbranch_scc1 Lsp16_%=\n"
    "s_barrier\n"
    "ds_read_b128 a[48:51], v192 offset:16384\n"
    "v_mul_f32 v48, v247, v48\n"
    "v_mul_f32 v49, v247, v49\n"
    "v_mul_f32 v50, v247, v50\n"
    "v_mul_f32 v51, v247, v51\n"
    "v_cvt_pk_bf16_f32 v240, v48, v49\n"
    "v_cvt_pk_bf16_f32 v241, v50, v51\n"
    "v_mfma_f32_16x16x32_bf16 v[100:103], a[112:115], v[172:175], 0\n"
    "s_add_u32 m0, s61, 32768\n"
    "ds_write_b64 v216, v[240:241] offset:128\n"
    "ds_read_b128 a[52:55], v193 offset:16384\n"
    "v_mul_f32 v52, v248, v52\n"
    "v_mul_f32 v53, v248, v53\n"
    "v_mul_f32 v54, v248, v54\n"
    "v_mul_f32 v55, v248, v55\n"
    "v_cvt_pk_bf16_f32 v242, v52, v53\n"
    "v_cvt_pk_bf16_f32 v243, v54, v55\n"
    "v_mfma_f32_16x16x32_bf16 v[104:107], a[112:115], v[176:179], 0\n"
    "s_sub_u32 s96, s96, 60\n"
    "buffer_load_dwordx4 v206, s[44:47], s57 offen offset:0 nt lds\n"
    "ds_write_b64 v216, v[242:243] offset:4224\n"
    "v_mul_f32 v56, v249, v56\n"
    "v_mul_f32 v57, v249, v57\n"
    "v_mul_f32 v58, v249, v58\n"
    "v_mul_f32 v59, v249, v59\n"
    "v_cvt_pk_bf16_f32 v244, v56, v57\n"
    "v_cvt_pk_bf16_f32 v245, v58, v59\n"
    "v_mfma_f32_16x16x32_bf16 v[0:3], a[80:83], v[168:171], 0\n"
    "s_max_i32 s96, s96, 0\n"
    "ds_write_b64 v216, v[244:245] offset:8320\n"
    "v_mul_f32 v60, v247, v60\n"
    "v_mul_f32 v61, v247, v61\n"
    "v_mul_f32 v62, v247, v62\n"
    "v_mul_f32 v63, v247, v63\n"
    "v_cvt_pk_bf16_f32 v240, v60, v61\n"
    "v_cvt_pk_bf16_f32 v241, v62, v63\n"
    "v_mfma_f32_16x16x32_bf16 v[4:7], a[80:83], v[172:175], 0\n"
    "s_add_u32 s97, s97, s96\n"
    "ds_write_b64 v217, v[240:241] offset:128\n"
    "ds_read_b128 a[56:59], v194 offset:16384\n"
    "v_mul_f32 v64, v248, v64\n"
    "v_mul_f32 v65, v248, v65\n"
    "v_mul_f32 v66, v248, v66\n"
    "v_mul_f32 v67, v248, v67\n"
    "v_cvt_pk_bf16_f32 v242, v64, v65\n"
    "v_cvt_pk_bf16_f32 v243, v66, v67\n"
    "v_mfma_f32_16x16x32_bf16 v[8:11], a[80:83], v[176:179], 0\n"
    "s_bfe_u32 s93, s76, 0x80008\n"
    "ds_write_b64 v217, v[242:243] offset:4224\n"
    "ds_read_b128 a[60:63], v195 offset:16384\n"
    "v_mul_f32 v68, v249, v68\n"
    "v_mul_f32 v69, v249, v69\n"
    "v_mul_f32 v70, v249, v70\n"
    "v_mul_f32 v71, v249, v71\n"
    "v_cvt_pk_bf16_f32 v244, v68, v69\n"
    "v_cvt_pk_bf16_f32 v245, v70, v71\n"
    "v_mfma_f32_16x16x32_bf16 v[12:15], a[84:87], v[168:171], 0\n"
    "s_add_u32 s97, s97, s93\n"
    "ds_write_b64 v217, v[244:245] offset:8320\n"
    "v_mfma_f32_16x16x32_bf16 v[16:19], a[84:87], v[172:175], 0\n"
    "ds_read_b128 a[64:67], v192 offset:20480\n"
    "v_mul_f32 v72, v247, v72\n"
    "v_mul_f32 v73, v247, v73\n"
    "v_mul_f32 v74, v247, v74\n"
    "v_mul_f32 v75, v247, v75\n"
    "v_cvt_pk_bf16_f32 v240, v72, v73\n"
    "v_cvt_pk_bf16_f32 v241, v74, v75\n"
    "v_mfma_f32_16x16x32_bf16 v[20:23], a[84:87], v[176:179], 0\n"
    "ds_write_b64 v218, v[240:241] offset:128\n"
    "ds_read_b128 a[68:71], v193 offset:20480\n"
    "v_mul_f32 v76, v248, v76\n"
    "v_mul_f32 v77, v248, v77\n"
    "v_mul_f32 v78, v248, v78\n"
    "v_mul_f32 v79, v248, v79\n"
    "v_cvt_pk_bf16_f32 v242, v76, v77\n"
    "v_cvt_pk_bf16_f32 v243, v78, v79\n"
    "v_mfma_f32_16x16x32_bf16 v[24:27], a[88:91], v[168:171], 0\n"
    "ds_write_b64 v218, v[242:243] offset:4224\n"
    "v_mul_f32 v80, v249, v80\n"
    "v_mul_f32 v81, v249, v81\n"
    "v_mul_f32 v82, v249, v82\n"
    "v_mul_f32 v83, v249, v83\n"
    "v_cvt_pk_bf16_f32 v244, v80, v81\n"
    "v_cvt_pk_bf16_f32 v245, v82, v83\n"
    "v_mfma_f32_16x16x32_bf16 v[28:31], a[88:91], v[172:175], 0\n"
    "buffer_load_dwordx4 v207, s[44:47], s57 offen offset:1024 nt lds\n"
    "ds_write_b64 v218, v[244:245] offset:8320\n"
    "v_mul_f32 v84, v247, v84\n"
    "v_mul_f32 v85, v247, v85\n"
    "v_mul_f32 v86, v247, v86\n"
    "v_mul_f32 v87, v247, v87\n"
    "v_cvt_pk_bf16_f32 v240, v84, v85\n"
    "v_cvt_pk_bf16_f32 v241, v86, v87\n"
    "v_mfma_f32_16x16x32_bf16 v[32:35], a[88:91], v[176:179], 0\n"
    "s_xor_b32 s57, s57, 8192\n"
    "ds_write_b64 v219, v[240:241] offset:128\n"
    "ds_read_b128 a[72:75], v194 offset:20480\n"
    "v_mul_f32 v88, v248, v88\n"
    "v_mul_f32 v89, v248, v89\n"
    "v_mul_f32 v90, v248, v90\n"
    "v_mul_f32 v91, v248, v91\n"
    "v_cvt_pk_bf16_f32 v242, v88, v89\n"
    "v_cvt_pk_bf16_f32 v243, v90, v91\n"
    "v_mfma_f32_16x16x32_bf16 v[36:39], a[92:95], v[168:171], 0\n"
    "s_add_u32 m0, s62, 0\n"
    "ds_write_b64 v219, v[242:243] offset:4224\n"
    "v_mul_f32 v92, v249, v92\n"
    "v_mul_f32 v93, v249, v93\n"
    "v_mul_f32 v94, v249, v94\n"
    "v_mul_f32 v95, v249, v95\n"
    "v_cvt_pk_bf16_f32 v244, v92, v93\n"
    "v_cvt_pk_bf16_f32 v245, v94, v95\n"
    "v_mfma_f32_16x16x32_bf16 v[40:43], a[92:95], v[172:175], 0\n"
    "ds_write_b64 v219, v[244:245] offset:8320\n"
    "ds_read_b128 a[76:79], v195 offset:20480\n"
    "v_mfma_f32_16x16x32_bf16 v[44:47], a[92:95], v[176:179], 0\n"
    "ds_read_b64_tr_b16 a[80:81], v196 offset:40960\n"
    "v_exp_f32 v144, v144\n"
    "v_mfma_f32_16x16x32_bf16 v[48:51], a[96:99], v[168:171], 0\n"
    "v_exp_f32 v145, v145\n"
    "v_mfma_f32_16x16x32_bf16 v[52:55], a[96:99], v[172:175], 0\n"
    "ds_read_b64_tr_b16 a[82:83], v196 offset:45056\n"
    "v_cvt_pk_bf16_f32 v180, v144, v145\n"
    "v_exp_f32 v146, v146\n"
    "v_mfma_f32_16x16x32_bf16 v[56:59], a[96:99], v[176:179], 0\n"
    "ds_read_b64_tr_b16 a[84:85], v197 offset:40960\n"
    "v_mfma_f32_16x16x32_bf16 v[60:63], a[100:103], v[168:171], 0\n"
    "buffer_load_dwordx4 v210, s[48:51], s58 offen offset:2048 nt lds\n"
    "v_exp_f32 v147, v147\n"
    "v_mfma_f32_16x16x32_bf16 v[64:67], a[100:103], v[172:175], 0\n"
    "s_add_u32 m0, s61, 8192\n"
    "v_cvt_pk_bf16_f32 v181, v146, v147\n"
    "v_exp_f32 v148, v148\n"
    "v_mfma_f32_16x16x32_bf16 v[68:71], a[100:103], v[176:179], 0\n"
    "ds_read_b64_tr_b16 a[86:87], v197 offset:45056\n"
    "v_mfma_f32_16x16x32_bf16 v[72:75], a[104:107], v[168:171], 0\n"
    "v_exp_f32 v149, v149\n"
    "v_mfma_f32_16x16x32_bf16 v[76:79], a[104:107], v[172:175], 0\n"
    "ds_read_b64_tr_b16 a[88:89], v198 offset:40960\n"
    "v_cvt_pk_bf16_f32 v182, v148, v149\n"
    "v_exp_f32 v150, v150\n"
    "v_mfma_f32_16x16x32_bf16 v[80:83], a[104:107], v[176:179], 0\n"
    "ds_read_b64_tr_b16 a[90:91], v198 offset:45056\n"
    "v_mfma_f32_16x16x32_bf16 v[84:87], a[108:111], v[168:171], 0\n"
    "v_exp_f32 v151, v151\n"
    "v_mfma_f32_16x16x32_bf16 v[88:91], a[108:111], v[172:175], 0\n"
    "ds_read_b64_tr_b16 a[92:93], v199 offset:40960\n"
    "v_cvt_pk_bf16_f32 v183, v150, v151\n"
    "v_exp_f32 v152, v152\n"
    "v_mfma_f32_16x16x32_bf16 v[92:95], a[108:111], v[176:179], 0\n"
    "buffer_load_dwordx4 v204, s[40:43], s56 offen offset:0 nt lds\n"
    "s_waitcnt lgkmcnt(4)\n"
    "v_mfma_f32_16x16x32_bf16 v[120:123], a[48:51], a[0:3], v[108:111]\n"
    "ds_read_b64_tr_b16 a[94:95], v199 offset:45056\n"
    "v_exp_f32 v153, v153\n"
    "v_mfma_f32_16x16x32_bf16 v[120:123], a[52:55], a[4:7], v[120:123]\n"
    "v_cvt_pk_bf16_f32 v184, v152, v153\n"
    "v_exp_f32 v154, v154\n"
    "v_mfma_f32_16x16x32_bf16 v[120:123], a[56:59], a[8:11], v[120:123]\n"
    "ds_read_b64_tr_b16 a[96:97], v200 offset:40960\n"
    "v_mfma_f32_16x16x32_bf16 v[120:123], a[60:63], a[12:15], v[120:123]\n"
    "ds_read_b64_tr_b16 a[98:99], v200 offset:45056\n"
    "v_exp_f32 v155, v155\n"
    "v_mfma_f32_16x16x32_bf16 v[124:127], a[64:67], a[0:3], v[108:111]\n"
    "v_cvt_pk_bf16_f32 v185, v154, v155\n"
    "v_exp_f32 v156, v156\n"
    "v_mfma_f32_16x16x32_bf16 v[124:127], a[68:71], a[4:7], v[124:127]\n"
    "ds_read_b64_tr_b16 a[100:101], v201 offset:40960\n"
    "v_exp_f32 v157, v157\n"
    "v_mfma_f32_16x16x32_bf16 v[124:127], a[72:75], a[8:11], v[124:127]\n"
    "v_cvt_pk_bf16_f32 v186, v156, v157\n"
    "v_mfma_f32_16x16x32_bf16 v[124:127], a[76:79], a[12:15], v[124:127]\n"
    "buffer_load_dwordx4 v205, s[40:43], s56 offen offset:1024 nt lds\n"
    "v_exp_f32 v158, v158\n"
    "v_mfma_f32_16x16x32_bf16 v[128:131], a[48:51], a[16:19], v[112:115]\n"
    "s_xor_b32 s56, s56, 24576\n"
    "ds_read_b64_tr_b16 a[102:103], v201 offset:45056\n"
    "v_exp_f32 v159, v159\n"
    "v_mfma_f32_16x16x32_bf16 v[128:131], a[52:55], a[20:23], v[128:131]\n"
    "s_add_u32 m0, s62, 0\n"
    "ds_read_b64_tr_b16 a[104:105], v202 offset:40960\n"
    "v_cvt_pk_bf16_f32 v187, v158, v159\n"
    "v_mfma_f32_16x16x32_bf16 v[128:131], a[56:59], a[24:27], v[128:131]\n"
    "v_exp_f32 v160, v160\n"
    "v_mfma_f32_16x16x32_bf16 v[128:131], a[60:63], a[28:31], v[128:131]\n"
    "ds_read_b64_tr_b16 a[106:107], v202 offset:45056\n"
    "v_exp_f32 v161, v161\n"
    "v_mfma_f32_16x16x32_bf16 v[132:135], a[64:67], a[16:19], v[112:115]\n"
    "ds_read_b64_tr_b16 a[108:109], v203 offset:40960\n"
    "v_cvt_pk_bf16_f32 v188, v160, v161\n"
    "v_mfma_f32_16x16x32_bf16 v[132:135], a[68:71], a[20:23], v[132:135]\n"
    "v_exp_f32 v162, v162\n"
    "v_mfma_f32_16x16x32_bf16 v[132:135], a[72:75], a[24:27], v[132:135]\n"
    "ds_read_b64_tr_b16 a[110:111], v203 offset:45056\n"
    "v_exp_f32 v163, v163\n"
    "v_mfma_f32_16x16x32_bf16 v[132:135], a[76:79], a[28:31], v[132:135]\n"
    "buffer_load_dwordx4 v211, s[48:51], s58 offen offset:3072 nt lds\n"
    "v_cvt_pk_bf16_f32 v189, v162, v163\n"
    "v_mfma_f32_16x16x32_bf16 v[136:139], a[48:51], a[32:35], v[116:119]\n"
    "v_exp_f32 v164, v164\n"
    "v_mfma_f32_16x16x32_bf16 v[136:139], a[52:55], a[36:39], v[136:139]\n"
    "v_exp_f32 v165, v165\n"
    "v_mfma_f32_16x16x32_bf16 v[136:139], a[56:59], a[40:43], v[136:139]\n"
    "v_cvt_pk_bf16_f32 v190, v164, v165\n"
    "v_mfma_f32_16x16x32_bf16 v[136:139], a[60:63], a[44:47], v[136:139]\n"
    "s_memrealtime s[94:95]\n"
    "v_exp_f32 v166, v166\n"
    "v_mfma_f32_16x16x32_bf16 v[140:143], a[64:67], a[32:35], v[116:119]\n"
    "v_exp_f32 v167, v167\n"
    "v_mfma_f32_16x16x32_bf16 v[140:143], a[68:71], a[36:39], v[140:143]\n"
    "v_cvt_pk_bf16_f32 v191, v166, v167\n"
    "v_mfma_f32_16x16x32_bf16 v[140:143], a[72:75], a[40:43], v[140:143]\n"
    "v_mfma_f32_16x16x32_bf16 v[140:143], a[76:79], a[44:47], v[140:143]\n"
    "s_branch Lloop_%=\n"
    "Ldrain_%=:\n"
    "s_waitcnt vmcnt(18) lgkmcnt(0)\n"
    "v_mfma_f32_16x16x32_bf16 v[96:99], a[112:115], v[168:171], v[96:99]\n"
    "Lpc17_%=:\n"
    "s_sub_u32 s96, s94, s97\n"
    "s_cmp_lt_i32 s96, 0\n"
    "s_cbranch_scc1 Lsp17_%=\n"
    "s_barrier\n"
    "ds_read_b64_tr_b16 a[164:165], v196 offset:57344\n"
    "ds_read_b64_tr_b16 a[166:167], v196 offset:61440\n"
    "v_mfma_f32_16x16x32_bf16 v[100:103], a[112:115], v[172:175], v[100:103]\n"
    "v_exp_f32 v144, v144\n"
    "v_mfma_f32_16x16x32_bf16 v[104:107], a[112:115], v[176:179], v[104:107]\n"
    "ds_read_b64_tr_b16 a[168:169], v197 offset:57344\n"
    "ds_read_b64_tr_b16 a[170:171], v197 offset:61440\n"
    "v_exp_f32 v145, v145\n"
    "v_mfma_f32_16x16x32_bf16 v[0:3], a[80:83], v[168:171], v[0:3]\n"
    "v_cvt_pk_bf16_f32 v180, v144, v145\n"
    "v_exp_f32 v146, v146\n"
    "v_mfma_f32_16x16x32_bf16 v[4:7], a[80:83], v[172:175], v[4:7]\n"
    "buffer_store_dwordx4 a[148:151], v222, s[52:55], s60 offen offset:0 sc0 sc1\n"
    "ds_read_b64_tr_b16 a[172:173], v198 offset:57344\n"
    "ds_read_b64_tr_b16 a[174:175], v198 offset:61440\n"
    "v_mfma_f32_16x16x32_bf16 v[8:11], a[80:83], v[176:179], v[8:11]\n"
    "v_exp_f32 v147, v147\n"
    "v_mfma_f32_16x16x32_bf16 v[12:15], a[84:87], v[168:171], v[12:15]\n"
    "ds_read_b64_tr_b16 a[176:177], v199 offset:57344\n"
    "ds_read_b64_tr_b16 a[178:179], v199 offset:61440\n"
    "v_cvt_pk_bf16_f32 v181, v146, v147\n"
    "v_exp_f32 v148, v148\n"
    "v_mfma_f32_16x16x32_bf16 v[16:19], a[84:87], v[172:175], v[16:19]\n"
    "v_exp_f32 v149, v149\n"
    "v_mfma_f32_16x16x32_bf16 v[20:23], a[84:87], v[176:179], v[20:23]\n"
    "ds_read_b64_tr_b16 a[180:181], v200 offset:57344\n"
    "ds_read_b64_tr_b16 a[182:183], v200 offset:61440\n"
    "v_cvt_pk_bf16_f32 v182, v148, v149\n"
    "v_mfma_f32_16x16x32_bf16 v[24:27], a[88:91], v[168:171], v[24:27]\n"
    "v_exp_f32 v150, v150\n"
    "v_mfma_f32_16x16x32_bf16 v[28:31], a[88:91], v[172:175], v[28:31]\n"
    "ds_read_b64_tr_b16 a[184:185], v201 offset:57344\n"
    "ds_read_b64_tr_b16 a[186:187], v201 offset:61440\n"
    "v_exp_f32 v151, v151\n"
    "v_mfma_f32_16x16x32_bf16 v[32:35], a[88:91], v[176:179], v[32:35]\n"
    "v_cvt_pk_bf16_f32 v183, v150, v151\n"
    "v_exp_f32 v152, v152\n"
    "v_mfma_f32_16x16x32_bf16 v[36:39], a[92:95], v[168:171], v[36:39]\n"
    "buffer_store_dwordx4 a[152:155], v222, s[52:55], s60 offen offset:1024 sc0 sc1\n"
    "ds_read_b64_tr_b16 a[188:189], v202 offset:57344\n"
    "ds_read_b64_tr_b16 a[190:191], v202 offset:61440\n"
    "v_mfma_f32_16x16x32_bf16 v[40:43], a[92:95], v[172:175], v[40:43]\n"
    "v_exp_f32 v153, v153\n"
    "v_mfma_f32_16x16x32_bf16 v[44:47], a[92:95], v[176:179], v[44:47]\n"
    "ds_read_b64_tr_b16 a[192:193], v203 offset:57344\n"
    "ds_read_b64_tr_b16 a[194:195], v203 offset:61440\n"
    "v_cvt_pk_bf16_f32 v184, v152, v153\n"
    "v_exp_f32 v154, v154\n"
    "v_mfma_f32_16x16x32_bf16 v[48:51], a[96:99], v[168:171], v[48:51]\n"
    "v_exp_f32 v155, v155\n"
    "v_mfma_f32_16x16x32_bf16 v[52:55], a[96:99], v[172:175], v[52:55]\n"
    "v_cvt_pk_bf16_f32 v185, v154, v155\n"
    "v_mfma_f32_16x16x32_bf16 v[56:59], a[96:99], v[176:179], v[56:59]\n"
    "v_exp_f32 v156, v156\n"
    "v_mfma_f32_16x16x32_bf16 v[60:63], a[100:103], v[168:171], v[60:63]\n"
    "v_exp_f32 v157, v157\n"
    "v_mfma_f32_16x16x32_bf16 v[64:67], a[100:103], v[172:175], v[64:67]\n"
    "v_cvt_pk_bf16_f32 v186, v156, v157\n"
    "v_exp_f32 v158, v158\n"
    "v_mfma_f32_16x16x32_bf16 v[68:71], a[100:103], v[176:179], v[68:71]\n"
    "buffer_store_dwordx4 a[156:159], v222, s[52:55], s60 offen offset:2048 sc0 sc1\n"
    "v_mfma_f32_16x16x32_bf16 v[72:75], a[104:107], v[168:171], v[72:75]\n"
    "v_exp_f32 v159, v159\n"
    "v_mfma_f32_16x16x32_bf16 v[76:79], a[104:107], v[172:175], v[76:79]\n"
    "v_cvt_pk_bf16_f32 v187, v158, v159\n"
    "v_exp_f32 v160, v160\n"
    "v_mfma_f32_16x16x32_bf16 v[80:83], a[104:107], v[176:179], v[80:83]\n"
    "v_exp_f32 v161, v161\n"
    "v_mfma_f32_16x16x32_bf16 v[84:87], a[108:111], v[168:171], v[84:87]\n"
    "v_cvt_pk_bf16_f32 v188, v160, v161\n"
    "v_mfma_f32_16x16x32_bf16 v[88:91], a[108:111], v[172:175], v[88:91]\n"
    "v_exp_f32 v162, v162\n"
    "v_mfma_f32_16x16x32_bf16 v[92:95], a[108:111], v[176:179], v[92:95]\n"
    "v_exp_f32 v163, v163\n"
    "v_mfma_f32_16x16x32_bf16 v[96:99], a[112:115], v[180:183], v[96:99]\n"
    "v_cvt_pk_bf16_f32 v189, v162, v163\n"
    "v_exp_f32 v164, v164\n"
    "s_waitcnt lgkmcnt(0)\n"
    "v_mfma_f32_16x16x32_bf16 v[0:3], a[164:167], v[180:183], v[0:3]\n"
    "buffer_store_dwordx4 a[160:163], v222, s[52:55], s60 offen offset:3072 sc0 sc1\n"
    "v_mfma_f32_16x16x32_bf16 v[12:15], a[168:171], v[180:183], v[12:15]\n"
    "s_add_u32 s52, s52, 12582912\n"
    "s_addc_u32 s53, s53, 0\n"
    "v_exp_f32 v165, v165\n"
    "v_mfma_f32_16x16x32_bf16 v[24:27], a[172:175], v[180:183], v[24:27]\n"
    "s_mov_b32 s54, 49152\n"
    "v_cvt_pk_bf16_f32 v190, v164, v165\n"
    "v_exp_f32 v166, v166\n"
    "v_mfma_f32_16x16x32_bf16 v[36:39], a[176:179], v[180:183], v[36:39]\n"
    "v_exp_f32 v167, v167\n"
    "v_mfma_f32_16x16x32_bf16 v[48:51], a[180:183], v[180:183], v[48:51]\n"
    "v_cvt_pk_bf16_f32 v191, v166, v167\n"
    "v_mfma_f32_16x16x32_bf16 v[60:63], a[184:187], v[180:183], v[60:63]\n"
    "v_mfma_f32_16x16x32_bf16 v[72:75], a[188:191], v[180:183], v[72:75]\n"
    "v_mfma_f32_16x16x32_bf16 v[84:87], a[192:195], v[180:183], v[84:87]\n"
    "v_mfma_f32_16x16x32_bf16 v[100:103], a[112:115], v[184:187], v[100:103]\n"
    "v_mfma_f32_16x16x32_bf16 v[4:7], a[164:167], v[184:187], v[4:7]\n"
    "v_rcp_f32 v247, v96\n"
    "v_cmp_nge_f32 s[68:69], s66, v96\n"
    "v_mfma_f32_16x16x32_bf16 v[16:19], a[168:171], v[184:187], v[16:19]\n"
    "v_mul_f32 v0, v247, v0\n"
    "v_mul_f32 v1, v247, v1\n"
    "v_mul_f32 v2, v247, v2\n"
    "v_mfma_f32_16x16x32_bf16 v[28:31], a[172:175], v[184:187], v[28:31]\n"
    "v_mul_f32 v3, v247, v3\n"
    "v_cvt_pk_bf16_f32 v240, v0, v1\n"
    "v_cvt_pk_bf16_f32 v241, v2, v3\n"
    "v_mfma_f32_16x16x32_bf16 v[40:43], a[176:179], v[184:187], v[40:43]\n"
    "ds_write_b64 v216, v[240:241] offset:0\n"
    "v_mul_f32 v12, v247, v12\n"
    "v_mul_f32 v13, v247, v13\n"
    "v_mul_f32 v14, v247, v14\n"
    "v_mfma_f32_16x16x32_bf16 v[52:55], a[180:183], v[184:187], v[52:55]\n"
    "v_mul_f32 v15, v247, v15\n"
    "v_cvt_pk_bf16_f32 v242, v12, v13\n"
    "v_cvt_pk_bf16_f32 v243, v14, v15\n"
    "v_mfma_f32_16x16x32_bf16 v[64:67], a[184:187], v[184:187], v[64:67]\n"
    "ds_write_b64 v217, v[242:243] offset:0\n"
    "v_mul_f32 v24, v247, v24\n"
    "v_mul_f32 v25, v247, v25\n"
    "v_mul_f32 v26, v247, v26\n"
    "v_mfma_f32_16x16x32_bf16 v[76:79], a[188:191], v[184:187], v[76:79]\n"
    "v_mul_f32 v27, v247, v27\n"
    "v_cvt_pk_bf16_f32 v244, v24, v25\n"
    "v_cvt_pk_bf16_f32 v245, v26, v27\n"
    "v_mfma_f32_16x16x32_bf16 v[88:91], a[192:195], v[184:187], v[88:91]\n"
    "ds_write_b64 v218, v[244:245] offset:0\n"
    "v_mul_f32 v36, v247, v36\n"
    "v_mul_f32 v37, v247, v37\n"
    "v_mul_f32 v38, v247, v38\n"
    "v_mfma_f32_16x16x32_bf16 v[104:107], a[112:115], v[188:191], v[104:107]\n"
    "v_mul_f32 v39, v247, v39\n"
    "v_cvt_pk_bf16_f32 v240, v36, v37\n"
    "v_cvt_pk_bf16_f32 v241, v38, v39\n"
    "v_mfma_f32_16x16x32_bf16 v[8:11], a[164:167], v[188:191], v[8:11]\n"
    "ds_write_b64 v219, v[240:241] offset:0\n"
    "v_mul_f32 v48, v247, v48\n"
    "v_mul_f32 v49, v247, v49\n"
    "v_mul_f32 v50, v247, v50\n"
    "v_mfma_f32_16x16x32_bf16 v[20:23], a[168:171], v[188:191], v[20:23]\n"
    "v_mul_f32 v51, v247, v51\n"
    "v_cvt_pk_bf16_f32 v242, v48, v49\n"
    "v_cvt_pk_bf16_f32 v243, v50, v51\n"
    "v_mfma_f32_16x16x32_bf16 v[32:35], a[172:175], v[188:191], v[32:35]\n"
    "ds_write_b64 v216, v[242:243] offset:128\n"
    "v_mul_f32 v60, v247, v60\n"
    "v_mul_f32 v61, v247, v61\n"
    "v_mul_f32 v62, v247, v62\n"
    "v_mfma_f32_16x16x32_bf16 v[44:47], a[176:179], v[188:191], v[44:47]\n"
    "v_mul_f32 v63, v247, v63\n"
    "v_cvt_pk_bf16_f32 v244, v60, v61\n"
    "v_cvt_pk_bf16_f32 v245, v62, v63\n"
    "v_mfma_f32_16x16x32_bf16 v[56:59], a[180:183], v[188:191], v[56:59]\n"
    "ds_write_b64 v217, v[244:245] offset:128\n"
    "v_mul_f32 v72, v247, v72\n"
    "v_mul_f32 v73, v247, v73\n"
    "v_mul_f32 v74, v247, v74\n"
    "v_mfma_f32_16x16x32_bf16 v[68:71], a[184:187], v[188:191], v[68:71]\n"
    "v_mul_f32 v75, v247, v75\n"
    "v_cvt_pk_bf16_f32 v240, v72, v73\n"
    "v_cvt_pk_bf16_f32 v241, v74, v75\n"
    "v_mfma_f32_16x16x32_bf16 v[80:83], a[188:191], v[188:191], v[80:83]\n"
    "ds_write_b64 v218, v[240:241] offset:128\n"
    "v_mul_f32 v84, v247, v84\n"
    "v_mul_f32 v85, v247, v85\n"
    "v_mul_f32 v86, v247, v86\n"
    "v_mfma_f32_16x16x32_bf16 v[92:95], a[192:195], v[188:191], v[92:95]\n"
    "v_mul_f32 v87, v247, v87\n"
    "v_cvt_pk_bf16_f32 v242, v84, v85\n"
    "v_cvt_pk_bf16_f32 v243, v86, v87\n"
    "ds_write_b64 v219, v[242:243] offset:128\n"
    "s_waitcnt lgkmcnt(0)\n"
    "ds_read_b128 a[116:119], v220 offset:0\n"
    "ds_read_b128 a[120:123], v221 offset:1024\n"
    "ds_read_b128 a[124:127], v220 offset:2048\n"
    "ds_read_b128 a[128:131], v221 offset:3072\n"
    "v_rcp_f32 v248, v100\n"
    "v_cmp_nge_f32 s[70:71], s66, v100\n"
    "v_mul_f32 v4, v248, v4\n"
    "v_mul_f32 v5, v248, v5\n"
    "v_mul_f32 v6, v248, v6\n"
    "v_mul_f32 v7, v248, v7\n"
    "v_cvt_pk_bf16_f32 v244, v4, v5\n"
    "v_cvt_pk_bf16_f32 v245, v6, v7\n"
    "v_mul_f32 v16, v248, v16\n"
    "v_mul_f32 v17, v248, v17\n"
    "v_mul_f32 v18, v248, v18\n"
    "v_mul_f32 v19, v248, v19\n"
    "v_cvt_pk_bf16_f32 v240, v16, v17\n"
    "v_cvt_pk_bf16_f32 v241, v18, v19\n"
    "ds_write_b64 v216, v[244:245] offset:4096\n"
    "ds_write_b64 v217, v[240:241] offset:4096\n"
    "v_mul_f32 v28, v248, v28\n"
    "v_mul_f32 v29, v248, v29\n"
    "v_mul_f32 v30, v248, v30\n"
    "v_mul_f32 v31, v248, v31\n"
    "v_cvt_pk_bf16_f32 v242, v28, v29\n"
    "v_cvt_pk_bf16_f32 v243, v30, v31\n"
    "v_mul_f32 v40, v248, v40\n"
    "v_mul_f32 v41, v248, v41\n"
    "v_mul_f32 v42, v248, v42\n"
    "v_mul_f32 v43, v248, v43\n"
    "v_cvt_pk_bf16_f32 v244, v40, v41\n"
    "v_cvt_pk_bf16_f32 v245, v42, v43\n"
    "ds_write_b64 v218, v[242:243] offset:4096\n"
    "ds_write_b64 v219, v[244:245] offset:4096\n"
    "s_waitcnt lgkmcnt(4)\n"
    "buffer_store_dwordx4 a[116:119], v222, s[52:55], s58 offen offset:0 sc0 sc1\n"
    "buffer_store_dwordx4 a[120:123], v222, s[52:55], s58 offen offset:1024 sc0 sc1\n"
    "buffer_store_dwordx4 a[124:127], v222, s[52:55], s58 offen offset:2048 sc0 sc1\n"
    "buffer_store_dwordx4 a[128:131], v222, s[52:55], s58 offen offset:3072 sc0 sc1\n"
    "v_mul_f32 v52, v248, v52\n"
    "v_mul_f32 v53, v248, v53\n"
    "v_mul_f32 v54, v248, v54\n"
    "v_mul_f32 v55, v248, v55\n"
    "v_cvt_pk_bf16_f32 v240, v52, v53\n"
    "v_cvt_pk_bf16_f32 v241, v54, v55\n"
    "v_mul_f32 v64, v248, v64\n"
    "v_mul_f32 v65, v248, v65\n"
    "v_mul_f32 v66, v248, v66\n"
    "v_mul_f32 v67, v248, v67\n"
    "v_cvt_pk_bf16_f32 v242, v64, v65\n"
    "v_cvt_pk_bf16_f32 v243, v66, v67\n"
    "ds_write_b64 v216, v[240:241] offset:4224\n"
    "ds_write_b64 v217, v[242:243] offset:4224\n"
    "v_mul_f32 v76, v248, v76\n"
    "v_mul_f32 v77, v248, v77\n"
    "v_mul_f32 v78, v248, v78\n"
    "v_mul_f32 v79, v248, v79\n"
    "v_cvt_pk_bf16_f32 v244, v76, v77\n"
    "v_cvt_pk_bf16_f32 v245, v78, v79\n"
    "v_mul_f32 v88, v248, v88\n"
    "v_mul_f32 v89, v248, v89\n"
    "v_mul_f32 v90, v248, v90\n"
    "v_mul_f32 v91, v248, v91\n"
    "v_cvt_pk_bf16_f32 v240, v88, v89\n"
    "v_cvt_pk_bf16_f32 v241, v90, v91\n"
    "ds_write_b64 v218, v[244:245] offset:4224\n"
    "ds_write_b64 v219, v[240:241] offset:4224\n"
    "s_waitcnt lgkmcnt(0)\n"
    "ds_read_b128 a[132:135], v220 offset:4096\n"
    "ds_read_b128 a[136:139], v221 offset:5120\n"
    "ds_read_b128 a[140:143], v220 offset:6144\n"
    "ds_read_b128 a[144:147], v221 offset:7168\n"
    "v_rcp_f32 v249, v104\n"
    "v_cmp_nge_f32 s[72:73], s66, v104\n"
    "v_mul_f32 v8, v249, v8\n"
    "v_mul_f32 v9, v249, v9\n"
    "v_mul_f32 v10, v249, v10\n"
    "v_mul_f32 v11, v249, v11\n"
    "v_cvt_pk_bf16_f32 v242, v8, v9\n"
    "v_cvt_pk_bf16_f32 v243, v10, v11\n"
    "v_mul_f32 v20, v249, v20\n"
    "v_mul_f32 v21, v249, v21\n"
    "v_mul_f32 v22, v249, v22\n"
    "v_mul_f32 v23, v249, v23\n"
    "v_cvt_pk_bf16_f32 v244, v20, v21\n"
    "v_cvt_pk_bf16_f32 v245, v22, v23\n"
    "ds_write_b64 v216, v[242:243] offset:8192\n"
    "ds_write_b64 v217, v[244:245] offset:8192\n"
    "v_mul_f32 v32, v249, v32\n"
    "v_mul_f32 v33, v249, v33\n"
    "v_mul_f32 v34, v249, v34\n"
    "v_mul_f32 v35, v249, v35\n"
    "v_cvt_pk_bf16_f32 v240, v32, v33\n"
    "v_cvt_pk_bf16_f32 v241, v34, v35\n"
    "v_mul_f32 v44, v249, v44\n"
    "v_mul_f32 v45, v249, v45\n"
    "v_mul_f32 v46, v249, v46\n"
    "v_mul_f32 v47, v249, v47\n"
    "v_cvt_pk_bf16_f32 v242, v44, v45\n"
    "v_cvt_pk_bf16_f32 v243, v46, v47\n"
    "ds_write_b64 v218, v[240:241] offset:8192\n"
    "ds_write_b64 v219, v[242:243] offset:8192\n"
    "s_waitcnt lgkmcnt(4)\n"
    "buffer_store_dwordx4 a[132:135], v222, s[52:55], s59 offen offset:0 sc0 sc1\n"
    "buffer_store_dwordx4 a[136:139], v222, s[52:55], s59 offen offset:1024 sc0 sc1\n"
    "buffer_store_dwordx4 a[140:143], v222, s[52:55], s59 offen offset:2048 sc0 sc1\n"
    "buffer_store_dwordx4 a[144:147], v222, s[52:55], s59 offen offset:3072 sc0 sc1\n"
    "v_mul_f32 v56, v249, v56\n"
    "v_mul_f32 v57, v249, v57\n"
    "v_mul_f32 v58, v249, v58\n"
    "v_mul_f32 v59, v249, v59\n"
    "v_cvt_pk_bf16_f32 v244, v56, v57\n"
    "v_cvt_pk_bf16_f32 v245, v58, v59\n"
    "v_mul_f32 v68, v249, v68\n"
    "v_mul_f32 v69, v249, v69\n"
    "v_mul_f32 v70, v249, v70\n"
    "v_mul_f32 v71, v249, v71\n"
    "v_cvt_pk_bf16_f32 v240, v68, v69\n"
    "v_cvt_pk_bf16_f32 v241, v70, v71\n"
    "ds_write_b64 v216, v[244:245] offset:8320\n"
    "ds_write_b64 v217, v[240:241] offset:8320\n"
    "v_mul_f32 v80, v249, v80\n"
    "v_mul_f32 v81, v249, v81\n"
    "v_mul_f32 v82, v249, v82\n"
    "v_mul_f32 v83, v249, v83\n"
    "v_cvt_pk_bf16_f32 v242, v80, v81\n"
    "v_cvt_pk_bf16_f32 v243, v82, v83\n"
    "v_mul_f32 v92, v249, v92\n"
    "v_mul_f32 v93, v249, v93\n"
    "v_mul_f32 v94, v249, v94\n"
    "v_mul_f32 v95, v249, v95\n"
    "v_cvt_pk_bf16_f32 v244, v92, v93\n"
    "v_cvt_pk_bf16_f32 v245, v94, v95\n"
    "ds_write_b64 v218, v[242:243] offset:8320\n"
    "ds_write_b64 v219, v[244:245] offset:8320\n"
    "s_waitcnt lgkmcnt(0)\n"
    "ds_read_b128 a[148:151], v220 offset:8192\n"
    "ds_read_b128 a[152:155], v221 offset:9216\n"
    "ds_read_b128 a[156:159], v220 offset:10240\n"
    "ds_read_b128 a[160:163], v221 offset:11264\n"
    "s_or_b64 s[68:69], s[68:69], s[70:71]\n"
    "s_lshl_b32 s64, s64, 1\n"
    "s_or_b64 s[68:69], s[68:69], s[72:73]\n"
    "s_cselect_b32 s67, 1, 0\n"
    "s_or_b32 s64, s64, s67\n"
    "s_waitcnt lgkmcnt(3)\n"
    "buffer_store_dwordx4 a[148:151], v222, s[52:55], s60 offen offset:0 sc0 sc1\n"
    "s_waitcnt lgkmcnt(2)\n"
    "buffer_store_dwordx4 a[152:155], v222, s[52:55], s60 offen offset:1024 sc0 sc1\n"
    "s_waitcnt lgkmcnt(1)\n"
    "buffer_store_dwordx4 a[156:159], v222, s[52:55], s60 offen offset:2048 sc0 sc1\n"
    "s_waitcnt lgkmcnt(0)\n"
    "buffer_store_dwordx4 a[160:163], v222, s[52:55], s60 offen offset:3072 sc0 sc1\n"
    "s_cmp_lg_u32 s64, 0\n"
    "s_cbranch_scc1 Lredo_%=\n"
    "s_endpgm\n"
    "Lredo_%=:\n"
    "s_waitcnt vmcnt(0)\n"
    "s_mov_b32 %[flags], s64\n"
    "s_branch Lend_%=\n"
    "Lsp0_%=:\n"
    "s_memrealtime s[94:95]\n"
    "s_waitcnt lgkmcnt(0)\n"
    "s_branch Lpc0_%=\n"
    "Lsp1_%=:\n"
    "s_memrealtime s[94:95]\n"
    "s_waitcnt lgkmcnt(0)\n"
    "s_branch Lpc1_%=\n"
    "Lsp2_%=:\n"
    "s_memrealtime s[94:95]\n"
    "s_waitcnt lgkmcnt(0)\n"
    "s_branch Lpc2_%=\n"
    "Lsp3_%=:\n"
    "s_memrealtime s[94:95]\n"
    "s_waitcnt lgkmcnt(0)\n"
    "s_branch Lpc3_%=\n"
    "Lsp4_%=:\n"
    "s_memrealtime s[94:95]\n"
    "s_waitcnt lgkmcnt(0)\n"
    "s_branch Lpc4_%=\n"
    "Lsp5_%=:\n"
    "s_memrealtime s[94:95]\n"
    "s_waitcnt lgkmcnt(0)\n"
    "s_branch Lpc5_%=\n"
    "Lsp6_%=:\n"
    "s_memrealtime s[94:95]\n"
    "s_waitcnt lgkmcnt(0)\n"
    "s_branch Lpc6_%=\n"
    "Lsp7_%=:\n"
    "s_memrealtime s[94:95]\n"
    "s_waitcnt lgkmcnt(0)\n"
    "s_branch Lpc7_%=\n"
    "Lsp8_%=:\n"
    "s_memrealtime s[94:95]\n"
    "s_waitcnt lgkmcnt(0)\n"
    "s_branch Lpc8_%=\n"
    "Lsp9_%=:\n"
    "s_memrealtime s[94:95]\n"
    "s_waitcnt lgkmcnt(0)\n"
    "s_branch Lpc9_%=\n"
    "Lsp10_%=:\n"
    "s_memrealtime s[94:95]\n"
    "s_waitcnt lgkmcnt(0)\n"
    "s_branch Lpc10_%=\n"
    "Lsp11_%=:\n"
    "s_memrealtime s[94:95]\n"
    "s_waitcnt lgkmcnt(0)\n"
    "s_branch Lpc11_%=\n"
    "Lsp12_%=:\n"
    "s_memrealtime s[94:95]\n"
    "s_waitcnt lgkmcnt(0)\n"
    "s_branch Lpc12_%=\n"
    "Lsp13_%=:\n"
    "s_memrealtime s[94:95]\n"
    "s_waitcnt lgkmcnt(0)\n"
    "s_branch Lpc13_%=\n"
    "Lsp14_%=:\n"
    "s_memrealtime s[94:95]\n"
    "s_waitcnt lgkmcnt(0)\n"
    "s_branch Lpc14_%=\n"
    "Lsp15_%=:\n"
    "s_memrealtime s[94:95]\n"
    "s_waitcnt lgkmcnt(0)\n"
    "s_branch Lpc15_%=\n"
    "Lsp16_%=:\n"
    "s_memrealtime s[94:95]\n"
    "s_waitcnt lgkmcnt(0)\n"
    "s_branch Lpc16_%=\n"
    "Lsp17_%=:\n"
    "s_memrealtime s[94:95]\n"
    "s_waitcnt lgkmcnt(0)\n"
    "s_branch Lpc17_%=\n"
    "Lend_%=:\n"
    : [flags] "=s"(flags)
    : [ka] "s"(__builtin_amdgcn_kernarg_segment_ptr()), [wg] "s"(head0), [rot] "s"(rot),   [dph] "s"(((wg >> 3) * 0) >> 5), [w] "s"(w), [lds] "s"((unsigned)(size_t)lds)
    : "v0", "v1", "v2", "v3", "v4", "v5", "v6", "v7", "v8", "v9", "v10", "v11", "v12", "v13", "v14", "v15", "v16", "v17", "v18", "v19", "v20", "v21", "v22", "v23", "v24", "v25", "v26", "v27", "v28", "v29", "v30", "v31", "v32", "v33", "v34", "v35", "v36", "v37", "v38", "v39", "v40", "v41", "v42", "v43", "v44", "v45", "v46", "v47", "v48", "v49", "v50", "v51", "v52", "v53", "v54", "v55", "v56", "v57", "v58", "v59", "v60", "v61", "v62", "v63", "v64", "v65", "v66", "v67", "v68", "v69", "v70", "v71", "v72", "v73", "v74", "v75", "v76", "v77", "v78", "v79", "v80", "v81", "v82", "v83", "v84", "v85", "v86", "v87", "v88", "v89", "v90", "v91", "v92", "v93", "v94", "v95", "v96", "v97", "v98", "v99", "v100", "v101", "v102", "v103", "v104", "v105", "v106", "v107", "v108", "v109", "v110", "v111", "v112", "v113", "v114", "v115", "v116", "v117", "v118", "v119", "v120", "v121", "v122", "v123", "v124", "v125", "v126", "v127", "v128", "v129", "v130", "v131", "v132", "v133", "v134", "v135", "v136", "v137", "v138", "v139", "v140", "v141", "v142", "v143", "v144", "v145", "v146", "v147", "v148", "v149", "v150", "v151", "v152", "v153", "v154", "v155", "v156", "v157", "v158", "v159", "v160", "v161", "v162", "v163", "v164", "v165", "v166", "v167", "v168", "v169", "v170", "v171", "v172", "v173", "v174", "v175", "v176", "v177", "v178", "v179", "v180", "v181", "v182", "v183", "v184", "v185", "v186", "v187", "v188", "v189", "v190", "v191", "v192", "v193", "v194", "v195", "v196", "v197", "v198", "v199", "v200", "v201", "v202", "v203", "v204", "v205", "v206", "v207", "v208", "v209", "v210", "v211", "v212", "v213", "v214", "v215", "v216", "v217", "v218", "v219", "v220", "v221", "v222", "v223", "v224", "v225", "v226", "v227", "v228", "v229", "v230", "v231", "v232", "v233", "v234", "v235", "v236", "v237", "v238", "v239", "v240", "v241", "v242", "v243", "v244", "v245", "v246", "v247", "v248", "v249", "v250", "v251", "v252", "v253", "v254", "v255", "a0", "a1", "a2", "a3", "a4", "a5", "a6", "a7", "a8", "a9", "a10", "a11", "a12", "a13", "a14", "a15", "a16", "a17", "a18", "a19", "a20", "a21", "a22", "a23", "a24", "a25", "a26", "a27", "a28", "a29", "a30", "a31", "a32", "a33", "a34", "a35", "a36", "a37", "a38", "a39", "a40", "a41", "a42", "a43", "a44", "a45", "a46", "a47", "a48", "a49", "a50", "a51", "a52", "a53", "a54", "a55", "a56", "a57", "a58", "a59", "a60", "a61", "a62", "a63", "a64", "a65", "a66", "a67", "a68", "a69", "a70", "a71", "a72", "a73", "a74", "a75", "a76", "a77", "a78", "a79", "a80", "a81", "a82", "a83", "a84", "a85", "a86", "a87", "a88", "a89", "a90", "a91", "a92", "a93", "a94", "a95", "a96", "a97", "a98", "a99", "a100", "a101", "a102", "a103", "a104", "a105", "a106", "a107", "a108", "a109", "a110", "a111", "a112", "a113", "a114", "a115", "a116", "a117", "a118", "a119", "a120", "a121", "a122", "a123", "a124", "a125", "a126", "a127", "a128", "a129", "a130", "a131", "a132", "a133", "a134", "a135", "a136", "a137", "a138", "a139", "a140", "a141", "a142", "a143", "a144", "a145", "a146", "a147", "a148", "a149", "a150", "a151", "a152", "a153", "a154", "a155", "a156", "a157", "a158", "a159", "a160", "a161", "a162", "a163", "a164", "a165", "a166", "a167", "a168", "a169", "a170", "a171", "a172", "a173", "a174", "a175", "a176", "a177", "a178", "a179", "a180", "a181", "a182", "a183", "a184", "a185", "a186", "a187", "a188", "a189", "a190", "a191", "a192", "a193", "a194", "a195", "a196", "s40", "s41", "s42", "s43", "s44", "s45", "s46", "s47", "s48", "s49", "s50", "s51", "s52", "s53", "s54", "s55", "s56", "s57", "s58", "s59", "s60", "s61", "s62", "s63", "s64", "s65", "s66", "s67", "s68", "s69", "s70", "s71", "s72", "s73", "s74", "s75", "s76", "s77", "s78", "s79", "s80", "s81", "s82", "s83", "s84", "s85", "s86", "s87", "s88", "s89", "s90", "s91", "s92", "s93", "s94", "s95", "s96", "s97", "vcc", "scc", "memory");
  if (flags) {
    const int lane = __builtin_amdgcn_mbcnt_hi(-1, __builtin_amdgcn_mbcnt_lo(-1, 0));
    for (int h = 0; h < HPW; ++h) {
      if (!((flags >> (HPW - 1 - h)) & 1)) continue;
      const size_t head = head0 + (size_t)h * NWG;
      for (int qb = 0; qb < 3; ++qb) {
        const size_t row0 = head * SQ + w * 48 + qb * 16;
        exact_rows16(Q + row0 * D, K + head * SK * D, V + head * SK * D, O + row0 * D, lane);
      }
    }
  }
}

extern "C" void kernel_launch(void* const* d_in, const int* in_sizes, int n_in, void* d_out, int out_size, void* d_ws, size_t ws_size, hipStream_t stream) {
  attn_fwd_stream<<<NWG, 256, 0, stream>>>((const u16*)d_in[0], (const u16*)d_in[1], (const u16*)d_in[2], (u16*)d_out
    );
}
```
